```python
import torch

def _softmax(x, axis=-1):
    return torch.softmax(x, dim=axis)
def _gelu(x):
    return torch.nn.functional.gelu(x, approximate="tanh")
def _silu(x):
    return torch.nn.functional.silu(x)
def _sigmoid(x):
    return torch.sigmoid(x)
def _layernorm(x, axis=-1, eps=1e-5):
    m = x.mean(dim=axis, keepdim=True)
    v = x.var(dim=axis, keepdim=True, unbiased=False)
    return (x - m) / torch.sqrt(v + eps)
def _scatter_add(tgt, idx, src, axis=0):
    return tgt.clone().index_add_(axis, idx, src)
def _segment_sum(src, seg, n):
    out = torch.zeros((n,) + src.shape[1:], dtype=src.dtype)
    return out.index_add_(0, seg, src)
def _pad(x, pw):
    flat = tuple(v for pair in reversed(pw) for v in pair)
    return torch.nn.functional.pad(x, flat)

kernel_name = 'concat_heads__mha_block_cached__compressed_kv_cla__decode'

def setup_inputs():

    _w = (lambda a: (lambda t: t.to(torch.bfloat16) if t.is_floating_point() else t)(torch.as_tensor(a)))
    def _A_setup():
        B, Sq, D = 1, 1, 640
        H, Sk, Dh = 32, 8192, 192
        return {
            "x": _w((lambda *s: torch.randn(*s, dtype=torch.bfloat16) * 1.6)(B, Sq, D)),
            "k_cache": _w((lambda *s: torch.randn(*s, dtype=torch.bfloat16) * 1.6)(B, H, Sk, Dh)),
            "v_cache": _w((lambda *s: torch.randn(*s, dtype=torch.bfloat16) * 0.05)(B, H, Sk, Dh)),
            "wq": _w((lambda *s: torch.randn(*s, dtype=torch.bfloat16) * 1.6)(D, H * Dh) * (D ** -0.5)),
            "wo": _w((lambda *s: torch.randn(*s, dtype=torch.bfloat16) * 0.05)(H * Dh, D) * ((H * Dh) ** -0.5)),
        }
    _A_in = _A_setup()

    def _B_setup():
        B, H, Sq, Sk, D, M = 1, 32, 1, 8192, 192, 640
        Dc = 4 * D
        return {
            "x": _w((lambda *s: torch.randn(*s, dtype=torch.bfloat16) * 3.2)(B, Sq, M)),
            "c_kv": _w((lambda *s: torch.randn(*s, dtype=torch.bfloat16) * 0.1)(B, Sk, Dc)),
            "w_q": _w((lambda *s: torch.randn(*s, dtype=torch.bfloat16) * 6.4)(M, H, D) * (M ** -0.5)),
            "w_uk": _w((lambda *s: torch.randn(*s, dtype=torch.bfloat16) * 6.4)(Dc, H, D) * (Dc ** -0.5)),
            "w_uv": _w((lambda *s: torch.randn(*s, dtype=torch.bfloat16) * 0.1)(Dc, H, D) * (Dc ** -0.5)),
            "w_o": _w((lambda *s: torch.randn(*s, dtype=torch.bfloat16) * 0.1)(H, D, M) * ((H * D) ** -0.5)),
        }
    _B_in = _B_setup()

    return {
        "A_x": _A_in["x"],
        "A_k_cache": _A_in["k_cache"],
        "A_v_cache": _A_in["v_cache"],
        "A_wq": _A_in["wq"],
        "A_wo": _A_in["wo"],
        "B_x": _B_in["x"],
        "B_c_kv": _B_in["c_kv"],
        "B_w_q": _B_in["w_q"],
        "B_w_uk": _B_in["w_uk"],
        "B_w_uv": _B_in["w_uv"],
        "B_w_o": _B_in["w_o"],
    }

def reference(A_x, A_k_cache, A_v_cache, A_wq, A_wo, B_x, B_c_kv, B_w_q, B_w_uk, B_w_uv, B_w_o):
    def _A_ref(x, k_cache, v_cache, wq, wo):
        B, Sq, D = x.shape
        H, Dh = 32, 192
        q = (lambda x, *d: x.permute(*d))((x @ wq).reshape(B, Sq, H, Dh), 0, 2, 1, 3)
        scores = torch.einsum("bhqd,bhkd->bhqk", q, k_cache) * (Dh ** -0.5)
        p = _softmax(scores, axis=-1)
        o = torch.einsum("bhqk,bhkd->bhqd", p, v_cache)
        return (lambda x, *d: x.permute(*d))(o, 0, 2, 1, 3).reshape(B, Sq, H * Dh) @ wo

    def _B_ref(x, c_kv, w_q, w_uk, w_uv, w_o):
        B, Sq, M = x.shape[0], x.shape[1], x.shape[2]
        Sk = c_kv.shape[1]
        H, D = w_q.shape[1], w_q.shape[2]
        q = torch.einsum("bqm,mhd->bhqd", x, w_q)
        k = torch.einsum("bkc,chd->bhkd", c_kv, w_uk)
        v = torch.einsum("bkc,chd->bhkd", c_kv, w_uv)
        scores = torch.einsum("bhqd,bhkd->bhqk", q, k) * (D ** -0.5)
        off = Sk - Sq
        qi = torch.arange(Sq)[:, None]
        kj = torch.arange(Sk)[None, :]
        cmask = kj <= (qi + off)
        scores = torch.where(cmask[None, None], scores, -1e9)
        p = _softmax(scores, axis=-1)
        ctx = torch.einsum("bhqk,bhkd->bhqd", p, v)
        return torch.einsum("bhqd,hdm->bqm", ctx, w_o)

    a = _A_ref(A_x, A_k_cache, A_v_cache, A_wq, A_wo)
    b = _B_ref(B_x, B_c_kv, B_w_q, B_w_uk, B_w_uv, B_w_o)
    return torch.concatenate([a, b], axis=-1)
```

```cpp
#include <hip/hip_runtime.h>
#include <hip/hip_bf16.h>
#include <stdint.h>

typedef unsigned u32x4 __attribute__((ext_vector_type(4)));
typedef unsigned u32x2 __attribute__((ext_vector_type(2)));
typedef __bf16 bf16x2 __attribute__((ext_vector_type(2)));
typedef __bf16 bf16x8 __attribute__((ext_vector_type(8)));
typedef float f32x16 __attribute__((ext_vector_type(16)));
typedef float f32x4 __attribute__((ext_vector_type(4)));
typedef unsigned short u16;

#define DI __device__ __forceinline__

constexpr int NH = 32, SK = 8192, DH = 192, DM = 640, DC = 768, HD = NH * DH;
constexpr float SCALE = 0.07216878364870322f;
constexpr float LOG2E = 1.4426950408889634f;

constexpr int NSA = 32;
constexpr int NSB = 64;
constexpr int AREC = 200;
constexpr size_t WS_CNT = 0;
constexpr size_t WS_QA = 4096;
constexpr size_t WS_QB = WS_QA + HD * 2;
constexpr size_t WS_QABS = WS_QB + HD * 2;
constexpr size_t WS_APART = WS_QABS + (size_t)NH * DC * 4;
constexpr size_t WS_BML = WS_APART + (size_t)NH * NSA * AREC * 4;
constexpr size_t WS_BO = WS_BML + (size_t)NSB * NH * 2 * 4;
constexpr size_t WS_CTXP = WS_BO + (size_t)NSB * NH * DC * 4;
constexpr size_t WS_OA = WS_CTXP + (size_t)NH * 8 * DH * 4;
constexpr size_t WS_OUTP = WS_OA + (size_t)HD * 4;
constexpr size_t WS_END = WS_OUTP + (size_t)2 * NH * DM * 4;

DI float dot2(unsigned a, unsigned b, float c) {
  return __builtin_amdgcn_fdot2_f32_bf16(__builtin_bit_cast(bf16x2, a), __builtin_bit_cast(bf16x2, b), c, false);
}
DI float rbf(float x) { return (float)(__bf16)x; }
DI float bflo(unsigned v) { return __builtin_bit_cast(float, v << 16); }
DI float bfhi(unsigned v) { return __builtin_bit_cast(float, v & 0xffff0000u); }
DI u16 f2bf(float x) { return __builtin_bit_cast(u16, (__bf16)x); }
DI unsigned pack2(float lo, float hi) { return (unsigned)f2bf(lo) | ((unsigned)f2bf(hi) << 16); }
template <int CTRL> DI float dpp(float x) {
  return __builtin_bit_cast(float, __builtin_amdgcn_mov_dpp(__builtin_bit_cast(int, x), CTRL, 0xf, 0xf, true));
}
DI float row_sum16(float x) {
  x += dpp<0x128>(x); x += dpp<0x124>(x); x += dpp<0x122>(x); x += dpp<0x121>(x); return x;
}
DI float ex2(float x) { return __builtin_amdgcn_exp2f(x); }
template <typename T> DI T ntload(const void* p) { return __builtin_nontemporal_load(reinterpret_cast<const T*>(p)); }

__global__ void __launch_bounds__(256) k_qproj(const u16* __restrict__ xA, const u16* __restrict__ wA,
                                                const u16* __restrict__ xB, const u16* __restrict__ wB,
                                                unsigned char* __restrict__ ws) {
  __shared__ float red[4][64];
  const int b = blockIdx.x, tid = threadIdx.x, lane = tid & 63, wave = tid >> 6;
  if (b == 0 && tid < 64) reinterpret_cast<unsigned*>(ws + WS_CNT)[tid] = 0u;
  const bool isB = b >= 96;
  const u16* x = isB ? xB : xA;
  const u16* w = isB ? wB : wA;
  u16* qout = reinterpret_cast<u16*>(ws + (isB ? WS_QB : WS_QA));
  const int col0 = (isB ? b - 96 : b) * 64;
  const int cl = lane & 7, rl = lane >> 3;
  u32x4 wv[20];
  float xv[20];
#pragma unroll
  for (int i = 0; i < 20; ++i) {
    const int row = i * 32 + wave * 8 + rl;
    wv[i] = ntload<u32x4>(w + (size_t)row * HD + col0 + cl * 8);
    xv[i] = bflo((unsigned)x[row]);
  }
  float acc[8];
#pragma unroll
  for (int j = 0; j < 8; ++j) acc[j] = 0.f;
#pragma unroll
  for (int i = 0; i < 20; ++i) {
#pragma unroll
    for (int j = 0; j < 4; ++j) {
      acc[2 * j] = fmaf(xv[i], bflo(wv[i][j]), acc[2 * j]);
      acc[2 * j + 1] = fmaf(xv[i], bfhi(wv[i][j]), acc[2 * j + 1]);
    }
  }
#pragma unroll
  for (int j = 0; j < 8; ++j) {
    acc[j] += __shfl_xor(acc[j], 8);
    acc[j] += __shfl_xor(acc[j], 16);
    acc[j] += __shfl_xor(acc[j], 32);
  }
  if (lane < 8) {
#pragma unroll
    for (int j = 0; j < 8; ++j) red[wave][cl * 8 + j] = acc[j];
  }
  __syncthreads();
  if (tid < 64) {
    const float s = (red[0][tid] + red[1][tid]) + (red[2][tid] + red[3][tid]);
    qout[col0 + tid] = f2bf(s);
  }
}

__global__ void __launch_bounds__(256) k_qabs(const u16* __restrict__ wuk, unsigned char* __restrict__ ws) {
  const int tid = threadIdx.x, lane = tid & 63, wave = tid >> 6;
  const int gw = blockIdx.x * 4 + wave;
  const int hg = gw & 7, cr = gw >> 3;
  const int r = lane >> 4, c = lane & 15;
  const int h = hg * 4 + r;
  const u16* qB = reinterpret_cast<const u16*>(ws + WS_QB);
  float* qabs = reinterpret_cast<float*>(ws + WS_QABS);
  const u32x4 qa = *reinterpret_cast<const u32x4*>(qB + h * DH + 8 * c);
  const u32x2 qb = *reinterpret_cast<const u32x2*>(qB + h * DH + 128 + 4 * c);
  u32x4 ka[6]; u32x2 kb[6];
#pragma unroll
  for (int i = 0; i < 6; ++i) {
    const u16* row = wuk + ((size_t)(cr * 6 + i) * NH + h) * DH;
    ka[i] = ntload<u32x4>(row + 8 * c);
    kb[i] = ntload<u32x2>(row + 128 + 4 * c);
  }
#pragma unroll
  for (int i = 0; i < 6; ++i) {
    float s = 0.f;
#pragma unroll
    for (int j = 0; j < 4; ++j) s = dot2(ka[i][j], qa[j], s);
    s = dot2(kb[i][0], qb[0], s); s = dot2(kb[i][1], qb[1], s);
    s = row_sum16(s);
    if (c == 0) qabs[h * DC + cr * 6 + i] = s;
  }
}

DI void attnA_block(const u16* __restrict__ K, const u16* __restrict__ V, unsigned char* __restrict__ ws, int bid, float* lds) {
  const int tid = threadIdx.x, lane = tid & 63, wave = tid >> 6;
  const int h = bid >> 5, sp = bid & 31;
  const int r = lane >> 4, c = lane & 15;
  const u16* q = reinterpret_cast<const u16*>(ws + WS_QA) + h * DH;
  const u32x4 qa = *reinterpret_cast<const u32x4*>(q + 8 * c);
  const u32x2 qb = *reinterpret_cast<const u32x2*>(q + 128 + 4 * c);
  const size_t base = ((size_t)h * SK + sp * 256 + wave * 64 + r) * DH;
  const u16* kp = K + base;
  const u16* vp = V + base;
  constexpr int NT = 4;
  u32x4 ka[2][4], va[2][4]; u32x2 kb[2][4], vb[2][4];
#define LOADT(buf, t) do { _Pragma("unroll") for (int i = 0; i < 4; ++i) { \
    const u16* kr = kp + (size_t)((t) * 16 + i * 4) * DH; const u16* vr = vp + (size_t)((t) * 16 + i * 4) * DH; \
    ka[buf][i] = ntload<u32x4>(kr + 8 * c); kb[buf][i] = ntload<u32x2>(kr + 128 + 4 * c); \
    va[buf][i] = ntload<u32x4>(vr + 8 * c); vb[buf][i] = ntload<u32x2>(vr + 128 + 4 * c); } } while (0)
  float m = -INFINITY, l = 0.f, acc[12];
#pragma unroll
  for (int j = 0; j < 12; ++j) acc[j] = 0.f;
  LOADT(0, 0);
  LOADT(1, 1);
#pragma unroll
  for (int t = 0; t < NT; ++t) {
    const int b = t & 1;
    float s[4];
#pragma unroll
    for (int i = 0; i < 4; ++i) {
      float d = 0.f;
#pragma unroll
      for (int j = 0; j < 4; ++j) d = dot2(ka[b][i][j], qa[j], d);
      d = dot2(kb[b][i][0], qb[0], d); d = dot2(kb[b][i][1], qb[1], d);
      d = row_sum16(d);
      s[i] = rbf(rbf(d) * SCALE);
    }
    const float mn = fmaxf(fmaxf(m, fmaxf(s[0], s[1])), fmaxf(s[2], s[3]));
    const float alpha = ex2((m - mn) * LOG2E);
    m = mn;
    float p[4];
#pragma unroll
    for (int i = 0; i < 4; ++i) p[i] = ex2((s[i] - mn) * LOG2E);
    l = l * alpha + ((p[0] + p[1]) + (p[2] + p[3]));
#pragma unroll
    for (int j = 0; j < 12; ++j) acc[j] *= alpha;
#pragma unroll
    for (int i = 0; i < 4; ++i) {
#pragma unroll
      for (int j = 0; j < 4; ++j) {
        acc[2 * j] = fmaf(p[i], bflo(va[b][i][j]), acc[2 * j]);
        acc[2 * j + 1] = fmaf(p[i], bfhi(va[b][i][j]), acc[2 * j + 1]);
      }
#pragma unroll
      for (int j = 0; j < 2; ++j) {
        acc[8 + 2 * j] = fmaf(p[i], bflo(vb[b][i][j]), acc[8 + 2 * j]);
        acc[8 + 2 * j + 1] = fmaf(p[i], bfhi(vb[b][i][j]), acc[8 + 2 * j + 1]);
      }
    }
    if (t + 2 < NT) LOADT(b, t + 2);
  }
#undef LOADT
  float M = fmaxf(m, __shfl_xor(m, 16)); M = fmaxf(M, __shfl_xor(M, 32));
  const float f = ex2((m - M) * LOG2E);
  l *= f; l += __shfl_xor(l, 16); l += __shfl_xor(l, 32);
#pragma unroll
  for (int j = 0; j < 12; ++j) { float a = acc[j] * f; a += __shfl_xor(a, 16); a += __shfl_xor(a, 32); acc[j] = a; }
  float* rec = lds + wave * AREC;
  if (r == 0) {
#pragma unroll
    for (int j = 0; j < 8; ++j) rec[8 + 8 * c + j] = acc[j];
#pragma unroll
    for (int j = 0; j < 4; ++j) rec[8 + 128 + 4 * c + j] = acc[8 + j];
    if (c == 0) { rec[0] = M; rec[1] = l; }
  }
  __syncthreads();
  if (tid < DH) {
    const float m0 = lds[0], m1 = lds[AREC], m2 = lds[2 * AREC], m3 = lds[3 * AREC];
    const float MM = fmaxf(fmaxf(m0, m1), fmaxf(m2, m3));
    const float f0 = ex2((m0 - MM) * LOG2E), f1 = ex2((m1 - MM) * LOG2E), f2 = ex2((m2 - MM) * LOG2E), f3 = ex2((m3 - MM) * LOG2E);
    float* out = reinterpret_cast<float*>(ws + WS_APART) + ((size_t)h * NSA + sp) * AREC;
    out[8 + tid] = (f0 * lds[8 + tid] + f1 * lds[AREC + 8 + tid]) + (f2 * lds[2 * AREC + 8 + tid] + f3 * lds[3 * AREC + 8 + tid]);
    if (tid == 0) {
      out[0] = MM;
      out[1] = (f0 * lds[1] + f1 * lds[AREC + 1]) + (f2 * lds[2 * AREC + 1] + f3 * lds[3 * AREC + 1]);
    }
  }
}

__global__ void __launch_bounds__(256) k_attnA(const u16* __restrict__ K, const u16* __restrict__ V, unsigned char* __restrict__ ws) {
  __shared__ float lds[4 * AREC];
  attnA_block(K, V, ws, blockIdx.x, lds);
}

#define MFMA32(a, b, c) __builtin_amdgcn_mfma_f32_32x32x16_bf16((a), (b), (c), 0, 0, 0)
DI bf16x8 pack8(const f32x16& x, int s) {
  u32x4 p;
#pragma unroll
  for (int j = 0; j < 4; ++j) p[j] = pack2(x[8 * s + 2 * j], x[8 * s + 2 * j + 1]);
  return __builtin_bit_cast(bf16x8, p);
}

__global__ void __launch_bounds__(512, 2) k_attnB(const u16* __restrict__ ckv, unsigned char* __restrict__ ws) {
  extern __shared__ __attribute__((aligned(16))) float dl[];
  float* red = dl;
  float* fin = dl + 8 * 4 * 1024;
  const int tid = threadIdx.x, lane = tid & 63, w = tid >> 6;
  const int cl = lane & 31, hh = lane >> 5;
  const int sp = blockIdx.x, key0 = sp * 128;
  const float* qabs = reinterpret_cast<const float*>(ws + WS_QABS);
  u32x4 cf[4][6];
#pragma unroll
  for (int t = 0; t < 4; ++t)
#pragma unroll
    for (int s = 0; s < 6; ++s)
      cf[t][s] = *reinterpret_cast<const u32x4*>(ckv + (size_t)(key0 + 32 * t + cl) * DC + 96 * w + 16 * s + 8 * hh);
  u32x4 qh[6], ql[6];
#pragma unroll
  for (int s = 0; s < 6; ++s) {
    const f32x4 a = *reinterpret_cast<const f32x4*>(qabs + cl * DC + 96 * w + 16 * s + 8 * hh);
    const f32x4 b = *reinterpret_cast<const f32x4*>(qabs + cl * DC + 96 * w + 16 * s + 8 * hh + 4);
    float v[8] = {a[0], a[1], a[2], a[3], b[0], b[1], b[2], b[3]};
#pragma unroll
    for (int j = 0; j < 4; ++j) {
      const float h0 = rbf(v[2 * j]), h1 = rbf(v[2 * j + 1]);
      qh[s][j] = pack2(h0, h1);
      ql[s][j] = pack2(v[2 * j] - h0, v[2 * j + 1] - h1);
    }
  }
  f32x16 zero;
#pragma unroll
  for (int i = 0; i < 16; ++i) zero[i] = 0.f;
#pragma unroll
  for (int t = 0; t < 4; ++t) {
    f32x16 S = zero;
#pragma unroll
    for (int s = 0; s < 6; ++s) {
      S = MFMA32(__builtin_bit_cast(bf16x8, cf[t][s]), __builtin_bit_cast(bf16x8, ql[s]), S);
    }
#pragma unroll
    for (int s = 0; s < 6; ++s) {
      S = MFMA32(__builtin_bit_cast(bf16x8, cf[t][s]), __builtin_bit_cast(bf16x8, qh[s]), S);
    }
#pragma unroll
    for (int g = 0; g < 4; ++g)
      *reinterpret_cast<f32x4*>(red + (((w * 4 + t) * 4 + g) * 64 + lane) * 4) = f32x4{S[4 * g], S[4 * g + 1], S[4 * g + 2], S[4 * g + 3]};
  }
  __syncthreads();
  {
    const int t = w >> 1;
#pragma unroll
    for (int gg = 0; gg < 2; ++gg) {
      const int g = 2 * (w & 1) + gg;
      f32x4 s = *reinterpret_cast<const f32x4*>(red + (((0 * 4 + t) * 4 + g) * 64 + lane) * 4);
#pragma unroll
      for (int ww = 1; ww < 8; ++ww) s += *reinterpret_cast<const f32x4*>(red + (((ww * 4 + t) * 4 + g) * 64 + lane) * 4);
      *reinterpret_cast<f32x4*>(fin + ((t * 4 + g) * 64 + lane) * 4) = s;
    }
  }
  __syncthreads();
  f32x16 P[4];
  float m = -INFINITY;
#pragma unroll
  for (int t = 0; t < 4; ++t)
#pragma unroll
    for (int g = 0; g < 4; ++g) {
      const f32x4 s = *reinterpret_cast<const f32x4*>(fin + ((t * 4 + g) * 64 + lane) * 4);
#pragma unroll
      for (int j = 0; j < 4; ++j) { const float v = rbf(rbf(s[j]) * SCALE); P[t][4 * g + j] = v; m = fmaxf(m, v); }
    }
  m = fmaxf(m, __shfl_xor(m, 32));
  float l = 0.f;
#pragma unroll
  for (int t = 0; t < 4; ++t)
#pragma unroll
    for (int i = 0; i < 16; ++i) { const float p = ex2((P[t][i] - m) * LOG2E); P[t][i] = p; l += p; }
  l += __shfl_xor(l, 32);
  u32x4 idlo, idhi;
#pragma unroll
  for (int j = 0; j < 4; ++j) {
    const int k0 = 8 * hh + 2 * j, k1 = k0 + 1;
    idlo[j] = (cl == k0 ? 0x3f80u : 0u) | (cl == k1 ? 0x3f800000u : 0u);
    idhi[j] = (cl == k0 + 16 ? 0x3f80u : 0u) | (cl == k1 + 16 ? 0x3f800000u : 0u);
  }
  f32x16 O[3];
#pragma unroll
  for (int ct = 0; ct < 3; ++ct) O[ct] = zero;
#pragma unroll
  for (int t = 0; t < 4; ++t) {
    const bf16x8 p0 = pack8(P[t], 0), p1 = pack8(P[t], 1);
#pragma unroll
    for (int ct = 0; ct < 3; ++ct) {
      f32x16 T = MFMA32(__builtin_bit_cast(bf16x8, cf[t][2 * ct]), __builtin_bit_cast(bf16x8, idlo), zero);
      T = MFMA32(__builtin_bit_cast(bf16x8, cf[t][2 * ct + 1]), __builtin_bit_cast(bf16x8, idhi), T);
      const bf16x8 a0 = pack8(T, 0), a1 = pack8(T, 1);
      O[ct] = MFMA32(a0, p0, O[ct]);
      O[ct] = MFMA32(a1, p1, O[ct]);
    }
  }
  float* bo = reinterpret_cast<float*>(ws + WS_BO) + ((size_t)sp * NH + cl) * DC + 96 * w;
#pragma unroll
  for (int ct = 0; ct < 3; ++ct)
#pragma unroll
    for (int g = 0; g < 4; ++g)
      *reinterpret_cast<f32x4*>(bo + 32 * ct + 8 * g + 4 * hh) = f32x4{O[ct][4 * g], O[ct][4 * g + 1], O[ct][4 * g + 2], O[ct][4 * g + 3]};
  if (w == 0 && lane < 32) {
    float* ml = reinterpret_cast<float*>(ws + WS_BML) + ((size_t)sp * NH + cl) * 2;
    ml[0] = m; ml[1] = l;
  }
}

__global__ void __launch_bounds__(256) k_mid(const u16* __restrict__ wuv, unsigned char* __restrict__ ws) {
  __shared__ float sh[4 * DH + 96 * 4 + 64];
  const int b = blockIdx.x, tid = threadIdx.x, lane = tid & 63, wave = tid >> 6;
  if (b >= 256) {
    const int h = b - 256;
    const float* part = reinterpret_cast<const float*>(ws + WS_APART) + (size_t)h * NSA * AREC;
    if (tid < DH) {
      float M = -INFINITY;
      for (int s = 0; s < NSA; ++s) M = fmaxf(M, part[s * AREC]);
      float L = 0.f, o = 0.f;
      for (int s = 0; s < NSA; ++s) {
        const float f = ex2((part[s * AREC] - M) * LOG2E);
        L = fmaf(f, part[s * AREC + 1], L);
        o = fmaf(f, part[s * AREC + 8 + tid], o);
      }
      reinterpret_cast<float*>(ws + WS_OA)[h * DH + tid] = rbf(o / L);
    }
    return;
  }
  const int h = b >> 3, cr = b & 7;
  const int r = lane >> 4, c = lane & 15;
  u32x4 wa[6]; u32x2 wb[6];
#pragma unroll
  for (int i = 0; i < 6; ++i) {
    const u16* row = wuv + ((size_t)(cr * 96 + wave * 24 + i * 4 + r) * NH + h) * DH;
    wa[i] = ntload<u32x4>(row + 8 * c);
    wb[i] = ntload<u32x2>(row + 128 + 4 * c);
  }
  float* wts = sh;
  float* cpart = sh + 64;
  float* red = sh + 64 + 384;
  const float* bml = reinterpret_cast<const float*>(ws + WS_BML);
  if (wave == 0) {
    const float mj = bml[((size_t)lane * NH + h) * 2], lj = bml[((size_t)lane * NH + h) * 2 + 1];
    float M = mj;
#pragma unroll
    for (int o = 1; o < 64; o <<= 1) M = fmaxf(M, __shfl_xor(M, o));
    const float f = ex2((mj - M) * LOG2E);
    float L = f * lj;
#pragma unroll
    for (int o = 1; o < 64; o <<= 1) L += __shfl_xor(L, o);
    wts[lane] = f / L;
  }
  __syncthreads();
  {
    const float* bo = reinterpret_cast<const float*>(ws + WS_BO);
    const int q4 = lane % 24, sh2 = lane / 24;
    f32x4 a = {0.f, 0.f, 0.f, 0.f};
    if (lane < 48) {
#pragma unroll
      for (int i = 0; i < 8; ++i) {
        const int s = wave * 16 + sh2 * 8 + i;
        const f32x4 v = *reinterpret_cast<const f32x4*>(bo + ((size_t)s * NH + h) * DC + cr * 96 + q4 * 4);
        a += wts[s] * v;
      }
    }
#pragma unroll
    for (int j = 0; j < 4; ++j) { const float o = __shfl(a[j], (lane + 24) & 63); if (lane < 24) cpart[wave * 96 + q4 * 4 + j] = a[j] + o; }
  }
  __syncthreads();
  float acc[12];
#pragma unroll
  for (int j = 0; j < 12; ++j) acc[j] = 0.f;
#pragma unroll
  for (int i = 0; i < 6; ++i) {
    const int cc = wave * 24 + i * 4 + r;
    const float p = (cpart[cc] + cpart[96 + cc]) + (cpart[192 + cc] + cpart[288 + cc]);
#pragma unroll
    for (int j = 0; j < 4; ++j) {
      acc[2 * j] = fmaf(p, bflo(wa[i][j]), acc[2 * j]);
      acc[2 * j + 1] = fmaf(p, bfhi(wa[i][j]), acc[2 * j + 1]);
    }
#pragma unroll
    for (int j = 0; j < 2; ++j) {
      acc[8 + 2 * j] = fmaf(p, bflo(wb[i][j]), acc[8 + 2 * j]);
      acc[8 + 2 * j + 1] = fmaf(p, bfhi(wb[i][j]), acc[8 + 2 * j + 1]);
    }
  }
#pragma unroll
  for (int j = 0; j < 12; ++j) { float a = acc[j]; a += __shfl_xor(a, 16); a += __shfl_xor(a, 32); acc[j] = a; }
  if (r == 0) {
#pragma unroll
    for (int j = 0; j < 8; ++j) red[wave * DH + 8 * c + j] = acc[j];
#pragma unroll
    for (int j = 0; j < 4; ++j) red[wave * DH + 128 + 4 * c + j] = acc[8 + j];
  }
  __syncthreads();
  if (tid < DH) {
    reinterpret_cast<float*>(ws + WS_CTXP)[((size_t)h * 8 + cr) * DH + tid] =
        (red[tid] + red[DH + tid]) + (red[2 * DH + tid] + red[3 * DH + tid]);
  }
}

__global__ void __launch_bounds__(256) k_out(const u16* __restrict__ woA, const u16* __restrict__ woB,
                                              unsigned char* __restrict__ ws, u16* __restrict__ out) {
  __shared__ float ov[DH];
  __shared__ float red[4][128];
  __shared__ unsigned lastf;
  const int b = blockIdx.x, tid = threadIdx.x, lane = tid & 63, wave = tid >> 6;
  const int nr = b % 5, h = (b / 5) & 31, mat = b / 160;
  const u16* W = (mat ? woB : woA) + (size_t)h * DH * DM + nr * 128;
  const int r = lane >> 4, c = lane & 15;
  u32x4 wv[12];
#pragma unroll
  for (int i = 0; i < 12; ++i) wv[i] = ntload<u32x4>(W + (size_t)(i * 16 + wave * 4 + r) * DM + 8 * c);
  if (tid < DH) {
    float v;
    if (mat == 0) v = reinterpret_cast<const float*>(ws + WS_OA)[h * DH + tid];
    else {
      const float* cp = reinterpret_cast<const float*>(ws + WS_CTXP) + (size_t)h * 8 * DH + tid;
      v = ((cp[0] + cp[DH]) + (cp[2 * DH] + cp[3 * DH])) + ((cp[4 * DH] + cp[5 * DH]) + (cp[6 * DH] + cp[7 * DH]));
      v = rbf(v);
    }
    ov[tid] = v;
  }
  __syncthreads();
  float acc[8];
#pragma unroll
  for (int j = 0; j < 8; ++j) acc[j] = 0.f;
#pragma unroll
  for (int i = 0; i < 12; ++i) {
    const float x = ov[i * 16 + wave * 4 + r];
#pragma unroll
    for (int j = 0; j < 4; ++j) {
      acc[2 * j] = fmaf(x, bflo(wv[i][j]), acc[2 * j]);
      acc[2 * j + 1] = fmaf(x, bfhi(wv[i][j]), acc[2 * j + 1]);
    }
  }
#pragma unroll
  for (int j = 0; j < 8; ++j) { acc[j] += __shfl_xor(acc[j], 16); acc[j] += __shfl_xor(acc[j], 32); }
  if (r == 0) {
#pragma unroll
    for (int j = 0; j < 8; ++j) red[wave][8 * c + j] = acc[j];
  }
  __syncthreads();
  float* outp = reinterpret_cast<float*>(ws + WS_OUTP) + (size_t)mat * NH * DM;
  if (tid < 128) outp[(size_t)h * DM + nr * 128 + tid] = (red[0][tid] + red[1][tid]) + (red[2][tid] + red[3][tid]);
  asm volatile("s_waitcnt vmcnt(0)" ::: "memory");
  __syncthreads();
  if (tid == 0) {
    __builtin_amdgcn_fence(__ATOMIC_RELEASE, "agent");
    asm volatile("s_waitcnt vmcnt(0)" ::: "memory");
    unsigned* cnt = reinterpret_cast<unsigned*>(ws + WS_CNT) + mat * 5 + nr;
    const unsigned old = __hip_atomic_fetch_add(cnt, 1u, __ATOMIC_RELAXED, __HIP_MEMORY_SCOPE_AGENT);
    const unsigned last = (old == (unsigned)(NH - 1)) ? 1u : 0u;
    if (last) { __builtin_amdgcn_fence(__ATOMIC_ACQUIRE, "agent"); asm volatile("s_waitcnt vmcnt(0)" ::: "memory"); }
    lastf = last;
  }
  __syncthreads();
  if (lastf == 0u) return;
  if (tid < 128) {
    float s = 0.f;
#pragma unroll 8
    for (int hh = 0; hh < NH; ++hh) s += outp[(size_t)hh * DM + nr * 128 + tid];
    out[mat * DM + nr * 128 + tid] = f2bf(s);
  }
}

extern "C" void kernel_launch(void* const* d_in, const int* in_sizes, int n_in,
                              void* d_out, int out_size, void* d_ws, size_t ws_size,
                              hipStream_t stream) {
  const u16* A_x = (const u16*)d_in[0];
  const u16* A_k = (const u16*)d_in[1];
  const u16* A_v = (const u16*)d_in[2];
  const u16* A_wq = (const u16*)d_in[3];
  const u16* A_wo = (const u16*)d_in[4];
  const u16* B_x = (const u16*)d_in[5];
  const u16* B_ckv = (const u16*)d_in[6];
  const u16* B_wq = (const u16*)d_in[7];
  const u16* B_wuk = (const u16*)d_in[8];
  const u16* B_wuv = (const u16*)d_in[9];
  const u16* B_wo = (const u16*)d_in[10];
  unsigned char* ws = (unsigned char*)d_ws;
  u16* out = (u16*)d_out;
  k_qproj<<<192, 256, 0, stream>>>(A_x, A_wq, B_x, B_wq, ws);
  k_qabs<<<256, 256, 0, stream>>>(B_wuk, ws);
  constexpr int LDSB = (8 * 4 * 1024 + 4 * 1024) * 4;
  (void)hipFuncSetAttribute((const void*)k_attnB, hipFuncAttributeMaxDynamicSharedMemorySize, LDSB);
  k_attnB<<<NSB, 512, LDSB, stream>>>(B_ckv, ws);
  k_attnA<<<NH * NSA, 256, 0, stream>>>(A_k, A_v, ws);
  k_mid<<<288, 256, 0, stream>>>(B_wuv, ws);
  k_out<<<320, 256, 0, stream>>>(A_wo, B_wo, ws, out);
}
```

```cpp
#include <hip/hip_runtime.h>
#include <hip/hip_bf16.h>
#include <stdint.h>

typedef unsigned u32x4 __attribute__((ext_vector_type(4)));
typedef unsigned u32x2 __attribute__((ext_vector_type(2)));
typedef __bf16 bf16x2 __attribute__((ext_vector_type(2)));
typedef __bf16 bf16x8 __attribute__((ext_vector_type(8)));
typedef float f32x16 __attribute__((ext_vector_type(16)));
typedef float f32x4 __attribute__((ext_vector_type(4)));
typedef unsigned short u16;
typedef unsigned long long u64;

#define DI __device__ __forceinline__
#define GAS __attribute__((address_space(1)))
#define LAS __attribute__((address_space(3)))
#define RLX_AGENT __ATOMIC_RELAXED, __HIP_MEMORY_SCOPE_AGENT
#define RLX_WG __ATOMIC_RELAXED, __HIP_MEMORY_SCOPE_WORKGROUP

constexpr int NH = 32, SK = 8192, DH = 192, DM = 640, DC = 768, HD = NH * DH;
constexpr float SCALE = 0.07216878364870322f;
constexpr float LOG2E = 1.4426950408889634f;
constexpr int NSB = 128;
constexpr unsigned kSpinCap = 1u << 18;

constexpr size_t Z_TMO = 0;
constexpr size_t Z_FQABS = 1024;
constexpr size_t Z_FBATT = 2048;
constexpr size_t Z_GQP = 4096;
constexpr size_t Z_GCTX = Z_GQP + (size_t)NH * 8 * 384 * 8;
constexpr size_t Z_GAP = Z_GCTX + (size_t)NH * 8 * 192 * 8;
constexpr size_t Z_GOUT = Z_GAP + (size_t)NH * 8 * 200 * 8;
constexpr size_t Z_END = Z_GOUT + (size_t)2 * 8 * NH * 80 * 8;
constexpr size_t W_QH = Z_END;
constexpr size_t W_QL = W_QH + (size_t)NH * DC * 2;
constexpr size_t W_BML = W_QL + (size_t)NH * DC * 2;
constexpr size_t W_BO = W_BML + (size_t)NH * NSB * 2 * 4;
constexpr size_t W_END = W_BO + (size_t)NSB * NH * DC * 4;
static_assert(Z_END % 16 == 0 && W_END < (1ull << 31), "ws layout");

constexpr int L_CTL = 0;
constexpr int L_QS = 64;
constexpr int L_AREC = 1024;
constexpr int L_RED = L_AREC + 3200;
constexpr int L_VEC = L_RED + 6144;
constexpr int L_CP = L_VEC + 768;
constexpr int L_R2 = L_CP + 1536;
constexpr int L_SP = 20480;
constexpr int L_SF = L_SP + 32768;
constexpr int L_BYTES = L_SF + 8192;
static_assert(L_R2 + 7680 <= L_SP, "lds layout");

DI float dot2(unsigned a, unsigned b, float c) {
  return __builtin_amdgcn_fdot2_f32_bf16(__builtin_bit_cast(bf16x2, a), __builtin_bit_cast(bf16x2, b), c, false);
}
DI float rbf(float x) { return (float)(__bf16)x; }
DI float asf(unsigned v) { return __builtin_bit_cast(float, v); }
DI float bflo(unsigned v) { return __builtin_bit_cast(float, v << 16); }
DI float bfhi(unsigned v) { return __builtin_bit_cast(float, v & 0xffff0000u); }
DI u16 f2bf(float x) { return __builtin_bit_cast(u16, (__bf16)x); }
DI unsigned pack2(float lo, float hi) { return (unsigned)f2bf(lo) | ((unsigned)f2bf(hi) << 16); }
template <int CTRL> DI float dpp(float x) {
  return __builtin_bit_cast(float, __builtin_amdgcn_mov_dpp(__builtin_bit_cast(int, x), CTRL, 0xf, 0xf, true));
}
DI float row_sum16(float x) {
  x += dpp<0x128>(x); x += dpp<0x124>(x); x += dpp<0x122>(x); x += dpp<0x121>(x); return x;
}
DI float ex2(float x) { return __builtin_amdgcn_exp2f(x); }
template <typename T> DI T ntload(const void* p) { return __builtin_nontemporal_load(reinterpret_cast<const T*>(p)); }
#define MFMA32(a, b, c) __builtin_amdgcn_mfma_f32_32x32x16_bf16(__builtin_bit_cast(bf16x8, a), __builtin_bit_cast(bf16x8, b), (c), 0, 0, 0)
DI u32x4 pack8(const f32x16& x, int s) {
  u32x4 p;
#pragma unroll
  for (int j = 0; j < 4; ++j) p[j] = pack2(x[8 * s + 2 * j], x[8 * s + 2 * j + 1]);
  return p;
}
#define LGKM0() asm volatile("s_waitcnt lgkmcnt(0)" ::: "memory")
#define VM0() asm volatile("s_waitcnt vmcnt(0)" ::: "memory")
#define CBAR() asm volatile("" ::: "memory")

DI void lds_arrive(LAS unsigned* cnt, int lane) {
  LGKM0();
  if (lane == 0) __hip_atomic_fetch_add(cnt, 1u, RLX_WG);
}
DI void lds_wait(LAS unsigned* cnt, unsigned target) {
  while ((unsigned)__builtin_amdgcn_readfirstlane(__hip_atomic_load(cnt, RLX_WG)) < target) __builtin_amdgcn_s_sleep(1);
  CBAR();
}
DI void st_granule(unsigned char* ws, size_t off, float v) {
  __hip_atomic_store((GAS u64*)(ws + off), (1ull << 32) | (u64)__builtin_bit_cast(unsigned, v), RLX_AGENT);
}
DI u64 ld_granule(unsigned char* ws, size_t off) { return __hip_atomic_load((GAS u64*)(ws + off), RLX_AGENT); }
DI bool spin_fail(unsigned& spins, unsigned char* ws) {
  __builtin_amdgcn_s_sleep(1);
  ++spins;
  if (spins > kSpinCap) { __hip_atomic_store((GAS unsigned*)(ws + Z_TMO), 1u, RLX_AGENT); return true; }
  if ((spins & 255u) == 0u) return __builtin_amdgcn_readfirstlane(__hip_atomic_load((GAS unsigned*)(ws + Z_TMO), RLX_AGENT)) != 0u;
  return false;
}
template <int N> DI void gather(unsigned char* ws, size_t off, size_t stride, bool active, float (&v)[N]) {
  for (unsigned spins = 0;;) {
    bool ok = true;
    if (active) {
#pragma unroll
      for (int k = 0; k < N; ++k) {
        const u64 x = ld_granule(ws, off + k * stride);
        v[k] = __builtin_bit_cast(float, (unsigned)x);
        ok &= (x >> 32) == 1ull;
      }
    }
    if (__all(ok)) break;
    if (spin_fail(spins, ws)) break;
  }
  CBAR();
}
DI void wait_flags(__amdgpu_buffer_rsrc_t rs, unsigned char* ws, int off, int nw, int lane) {
  for (unsigned spins = 0;;) {
    bool ok = true;
    if (lane * 4 < nw) {
      const u32x4 f = __builtin_amdgcn_raw_buffer_load_b128(rs, off + lane * 16, 0, 16);
      ok = (f[0] != 0u) & (f[1] != 0u) & (f[2] != 0u) & (f[3] != 0u);
    }
    if (__all(ok)) break;
    if (spin_fail(spins, ws)) break;
  }
  CBAR();
}

DI void a_role(const u16* __restrict__ K, const u16* __restrict__ V, LAS unsigned char* L, int h, int j, int aw, int lane) {
  const int r = lane >> 4, c = lane & 15;
  const size_t base = ((size_t)h * SK + j * 1024 + aw * 256 + r) * DH;
  const u16* kp = K + base;
  const u16* vp = V + base;
  constexpr int NT = 16;
  u32x4 ka[2][4], va[2][4]; u32x2 kb[2][4], vb[2][4];
#define LOADT(buf, t) do { _Pragma("unroll") for (int i = 0; i < 4; ++i) { \
    const u16* kr = kp + (size_t)((t) * 16 + i * 4) * DH; const u16* vr = vp + (size_t)((t) * 16 + i * 4) * DH; \
    ka[buf][i] = ntload<u32x4>(kr + 8 * c); kb[buf][i] = ntload<u32x2>(kr + 128 + 4 * c); \
    va[buf][i] = ntload<u32x4>(vr + 8 * c); vb[buf][i] = ntload<u32x2>(vr + 128 + 4 * c); } } while (0)
  LOADT(0, 0);
  LOADT(1, 1);
  __builtin_amdgcn_sched_barrier(0);
  lds_wait((LAS unsigned*)(L + L_CTL), 4u);
  const u32x4 qa = *(const LAS u32x4*)(L + L_QS + 16 * c);
  const u32x2 qb = *(const LAS u32x2*)(L + L_QS + 256 + 8 * c);
  float m = -INFINITY, l = 0.f, acc[12];
#pragma unroll
  for (int k = 0; k < 12; ++k) acc[k] = 0.f;
  for (int t0 = 0; t0 < NT; t0 += 2) {
#pragma unroll
    for (int b = 0; b < 2; ++b) {
      const int t = t0 + b;
      float s[4];
#pragma unroll
      for (int i = 0; i < 4; ++i) {
        float d = 0.f;
#pragma unroll
        for (int k = 0; k < 4; ++k) d = dot2(ka[b][i][k], qa[k], d);
        d = dot2(kb[b][i][0], qb[0], d); d = dot2(kb[b][i][1], qb[1], d);
        d = row_sum16(d);
        s[i] = rbf(rbf(d) * SCALE);
      }
      const float mn = fmaxf(fmaxf(m, fmaxf(s[0], s[1])), fmaxf(s[2], s[3]));
      const float alpha = ex2((m - mn) * LOG2E);
      m = mn;
      float p[4];
#pragma unroll
      for (int i = 0; i < 4; ++i) p[i] = ex2((s[i] - mn) * LOG2E);
      l = l * alpha + ((p[0] + p[1]) + (p[2] + p[3]));
#pragma unroll
      for (int k = 0; k < 12; ++k) acc[k] *= alpha;
#pragma unroll
      for (int i = 0; i < 4; ++i) {
#pragma unroll
        for (int k = 0; k < 4; ++k) {
          acc[2 * k] = fmaf(p[i], bflo(va[b][i][k]), acc[2 * k]);
          acc[2 * k + 1] = fmaf(p[i], bfhi(va[b][i][k]), acc[2 * k + 1]);
        }
#pragma unroll
        for (int k = 0; k < 2; ++k) {
          acc[8 + 2 * k] = fmaf(p[i], bflo(vb[b][i][k]), acc[8 + 2 * k]);
          acc[8 + 2 * k + 1] = fmaf(p[i], bfhi(vb[b][i][k]), acc[8 + 2 * k + 1]);
        }
      }
      if (t + 2 < NT) LOADT(b, t + 2);
    }
  }
#undef LOADT
  float M = fmaxf(m, __shfl_xor(m, 16)); M = fmaxf(M, __shfl_xor(M, 32));
  const float f = ex2((m - M) * LOG2E);
  l *= f; l += __shfl_xor(l, 16); l += __shfl_xor(l, 32);
#pragma unroll
  for (int k = 0; k < 12; ++k) { float a = acc[k] * f; a += __shfl_xor(a, 16); a += __shfl_xor(a, 32); acc[k] = a; }
  LAS float* rec = (LAS float*)(L + L_AREC) + aw * 200;
  if (r == 0) {
#pragma unroll
    for (int k = 0; k < 8; ++k) rec[8 + 8 * c + k] = acc[k];
#pragma unroll
    for (int k = 0; k < 4; ++k) rec[8 + 128 + 4 * c + k] = acc[8 + k];
    if (c == 0) { rec[0] = M; rec[1] = l; }
  }
  lds_arrive((LAS unsigned*)(L + L_CTL) + 1, lane);
}

DI void fma_row(float (&acc)[12], float p, const u32x4& a, const u32x2& b) {
#pragma unroll
  for (int k = 0; k < 4; ++k) { acc[2 * k] = fmaf(p, bflo(a[k]), acc[2 * k]); acc[2 * k + 1] = fmaf(p, bfhi(a[k]), acc[2 * k + 1]); }
#pragma unroll
  for (int k = 0; k < 2; ++k) { acc[8 + 2 * k] = fmaf(p, bflo(b[k]), acc[8 + 2 * k]); acc[8 + 2 * k + 1] = fmaf(p, bfhi(b[k]), acc[8 + 2 * k + 1]); }
}
DI void rows_to_lds(float (&acc)[12], LAS float* dst, int lane) {
  const int r = lane >> 4, c = lane & 15;
#pragma unroll
  for (int k = 0; k < 12; ++k) { float a = acc[k]; a += __shfl_xor(a, 16); a += __shfl_xor(a, 32); acc[k] = a; }
  if (r == 0) {
#pragma unroll
    for (int k = 0; k < 8; ++k) dst[8 * c + k] = acc[k];
#pragma unroll
    for (int k = 0; k < 4; ++k) dst[128 + 4 * c + k] = acc[8 + k];
  }
}

struct WoFrag { u32x4 w[8]; };
DI void wo_prefetch(WoFrag& f, const u16* __restrict__ W, int h, int j, int bw, int lane) {
  const int rr = lane / 10, cc = lane % 10;
  const int rs = lane < 60 ? rr : 0;
#pragma unroll
  for (int i = 0; i < 8; ++i)
    f.w[i] = ntload<u32x4>(W + (size_t)(h * DH + 48 * bw + 6 * i + rs) * DM + 80 * j + 8 * cc);
}
DI void wo_step(const WoFrag& f, LAS unsigned char* L, unsigned char* ws, u16* __restrict__ out, int mat, int hfin,
                int h, int j, int bw, int lane, unsigned& gen) {
  LAS unsigned* bc = (LAS unsigned*)(L + L_CTL) + 2;
  const int rr = lane / 10, cc = lane % 10;
  const int t = bw * 64 + lane;
  const LAS float* vec = (const LAS float*)(L + L_VEC);
  float acc[8];
#pragma unroll
  for (int k = 0; k < 8; ++k) acc[k] = 0.f;
  if (lane < 60) {
#pragma unroll
    for (int i = 0; i < 8; ++i) {
      const float x = vec[48 * bw + 6 * i + rr];
#pragma unroll
      for (int k = 0; k < 4; ++k) { acc[2 * k] = fmaf(x, bflo(f.w[i][k]), acc[2 * k]); acc[2 * k + 1] = fmaf(x, bfhi(f.w[i][k]), acc[2 * k + 1]); }
    }
    LAS float* r2 = (LAS float*)(L + L_R2) + (bw * 6 + rr) * 80 + 8 * cc;
#pragma unroll
    for (int k = 0; k < 8; ++k) r2[k] = acc[k];
  }
  lds_arrive(bc, lane); gen += 4; lds_wait(bc, gen);
  if (t < 80) {
    const LAS float* r2 = (const LAS float*)(L + L_R2) + t;
    float s = 0.f;
#pragma unroll
    for (int k = 0; k < 24; ++k) s += r2[k * 80];
    st_granule(ws, Z_GOUT + ((((size_t)mat * 8 + j) * NH + h) * 80 + t) * 8, s);
  }
  if (h == hfin && bw < 2) {
    float v0[16], v1[16];
    const size_t off = Z_GOUT + ((((size_t)mat * 8 + j) * NH) * 80 + t) * 8;
    gather<16>(ws, off, 640, t < 80, v0);
    gather<16>(ws, off + 16 * 640, 640, t < 80, v1);
    if (t < 80) {
      float s = 0.f;
#pragma unroll
      for (int k = 0; k < 16; ++k) s += v0[k];
#pragma unroll
      for (int k = 0; k < 16; ++k) s += v1[k];
      out[mat * DM + 80 * j + t] = f2bf(s);
    }
  }
  lds_arrive(bc, lane); gen += 4; lds_wait(bc, gen);
}

DI void b_role(const u16* __restrict__ xA, const u16* __restrict__ wqA, const u16* __restrict__ woA,
               const u16* __restrict__ xB, const u16* __restrict__ ckv, const u16* __restrict__ wqB,
               const u16* __restrict__ wuk, const u16* __restrict__ wuv, const u16* __restrict__ woB,
               unsigned char* ws, u16* __restrict__ out, LAS unsigned char* L, int h, int j, int bw, int lane) {
  const int r = lane >> 4, c = lane & 15;
  const int t = bw * 64 + lane;
  LAS unsigned* bc = (LAS unsigned*)(L + L_CTL) + 2;
  unsigned gen = 0;
  const __amdgpu_buffer_rsrc_t rs = __builtin_amdgcn_make_buffer_rsrc((void*)ws, 0, (int)W_END, 0x00020000);

  u32x4 ua[6]; u32x2 ub[6];
  {
    u32x4 wa[2][5]; u32x2 wb[2][5]; float xv[2][5];
#pragma unroll
    for (int i = 0; i < 5; ++i) {
      const int row = 80 * j + 20 * bw + 4 * i + r;
      const u16* pa = wqA + (size_t)row * HD + h * DH;
      const u16* pb = wqB + (size_t)row * HD + h * DH;
      wa[0][i] = ntload<u32x4>(pa + 8 * c); wb[0][i] = ntload<u32x2>(pa + 128 + 4 * c);
      wa[1][i] = ntload<u32x4>(pb + 8 * c); wb[1][i] = ntload<u32x2>(pb + 128 + 4 * c);
      xv[0][i] = bflo((unsigned)xA[row]); xv[1][i] = bflo((unsigned)xB[row]);
    }
#pragma unroll
    for (int i = 0; i < 6; ++i) {
      const u16* row = wuk + ((size_t)(96 * j + 24 * bw + 4 * i + r) * NH + h) * DH;
      ua[i] = ntload<u32x4>(row + 8 * c); ub[i] = ntload<u32x2>(row + 128 + 4 * c);
    }
    __builtin_amdgcn_sched_barrier(0);
#pragma unroll
    for (int mt = 0; mt < 2; ++mt) {
      float acc[12];
#pragma unroll
      for (int k = 0; k < 12; ++k) acc[k] = 0.f;
#pragma unroll
      for (int i = 0; i < 5; ++i) fma_row(acc, xv[mt][i], wa[mt][i], wb[mt][i]);
      rows_to_lds(acc, (LAS float*)(L + L_RED) + (bw * 2 + mt) * DH, lane);
    }
  }
  lds_arrive(bc, lane); gen += 4; lds_wait(bc, gen);
  {
    const LAS float* red = (const LAS float*)(L + L_RED);
    const size_t gq = Z_GQP + ((size_t)h * 8 + j) * 384 * 8;
    st_granule(ws, gq + (size_t)t * 8, (red[t] + red[384 + t]) + (red[768 + t] + red[1152 + t]));
    if (t < 128) st_granule(ws, gq + (size_t)(256 + t) * 8, (red[256 + t] + red[640 + t]) + (red[1024 + t] + red[1408 + t]));
    float v[8];
    gather<8>(ws, Z_GQP + (size_t)h * 8 * 384 * 8 + (size_t)t * 8, 384 * 8, true, v);
    ((LAS u16*)(L + L_QS))[t] = f2bf(((v[0] + v[1]) + (v[2] + v[3])) + ((v[4] + v[5]) + (v[6] + v[7])));
    if (bw < 2) {
      gather<8>(ws, Z_GQP + (size_t)h * 8 * 384 * 8 + (size_t)(256 + t) * 8, 384 * 8, true, v);
      ((LAS u16*)(L + L_QS))[256 + t] = f2bf(((v[0] + v[1]) + (v[2] + v[3])) + ((v[4] + v[5]) + (v[6] + v[7])));
    }
  }
  lds_arrive((LAS unsigned*)(L + L_CTL), lane);
  lds_wait((LAS unsigned*)(L + L_CTL), 4u);

  {
    const u32x4 qa = *(const LAS u32x4*)(L + L_QS + 384 + 16 * c);
    const u32x2 qb = *(const LAS u32x2*)(L + L_QS + 384 + 256 + 8 * c);
    LAS float* qv = (LAS float*)(L + L_VEC);
#pragma unroll
    for (int i = 0; i < 6; ++i) {
      float s = 0.f;
#pragma unroll
      for (int k = 0; k < 4; ++k) s = dot2(ua[i][k], qa[k], s);
      s = dot2(ub[i][0], qb[0], s); s = dot2(ub[i][1], qb[1], s);
      s = row_sum16(s);
      if (c == 0) qv[24 * bw + 4 * i + r] = s;
    }
  }
  lds_arrive(bc, lane); gen += 4; lds_wait(bc, gen);
  if (bw == 0) {
    if (lane < 24) {
      const int k8 = lane % 12, lo = lane / 12;
      const LAS float* qv = (const LAS float*)(L + L_VEC) + 8 * k8;
      u32x4 o;
#pragma unroll
      for (int k = 0; k < 4; ++k) {
        const float a = qv[2 * k], b = qv[2 * k + 1];
        const float ah = rbf(a), bh = rbf(b);
        o[k] = lo ? pack2(a - ah, b - bh) : pack2(ah, bh);
      }
      __builtin_amdgcn_raw_buffer_store_b128(o, rs, (int)((lo ? W_QL : W_QH) + ((size_t)h * DC + 96 * j + 8 * k8) * 2), 0, 16);
    }
    VM0();
    if (lane == 0) __hip_atomic_store((GAS unsigned*)(ws + Z_FQABS) + h * 8 + j, 1u, RLX_AGENT);
  }

  if ((j & 1) == 0) {
    const int sb = h * 4 + (j >> 1);
    const int key0 = sb * 64;
    const int cl = lane & 31, hh = lane >> 5;
    u32x4 cf[2][12];
#pragma unroll
    for (int tt = 0; tt < 2; ++tt)
#pragma unroll
      for (int s = 0; s < 12; ++s)
        cf[tt][s] = *reinterpret_cast<const u32x4*>(ckv + (size_t)(key0 + 32 * tt + cl) * DC + 192 * bw + 16 * s + 8 * hh);
    __builtin_amdgcn_sched_barrier(0);
    wait_flags(rs, ws, (int)Z_FQABS, 256, lane);
    f32x16 zero;
#pragma unroll
    for (int i = 0; i < 16; ++i) zero[i] = 0.f;
    f32x16 S0 = zero, S1 = zero;
    const int qoff = (cl * DC + 192 * bw + 8 * hh) * 2;
#pragma unroll
    for (int half = 0; half < 2; ++half) {
      u32x4 qh[6], ql[6];
#pragma unroll
      for (int s = 0; s < 6; ++s) {
        qh[s] = __builtin_amdgcn_raw_buffer_load_b128(rs, (int)W_QH + qoff + (half * 6 + s) * 32, 0, 16);
        ql[s] = __builtin_amdgcn_raw_buffer_load_b128(rs, (int)W_QL + qoff + (half * 6 + s) * 32, 0, 16);
      }
#pragma unroll
      for (int s = 0; s < 6; ++s) {
        S0 = MFMA32(cf[0][half * 6 + s], ql[s], S0);
        S1 = MFMA32(cf[1][half * 6 + s], ql[s], S1);
        S0 = MFMA32(cf[0][half * 6 + s], qh[s], S0);
        S1 = MFMA32(cf[1][half * 6 + s], qh[s], S1);
      }
    }
    LAS f32x4* sp = (LAS f32x4*)(L + L_SP);
#pragma unroll
    for (int g = 0; g < 4; ++g) {
      sp[((bw * 2 + 0) * 4 + g) * 64 + lane] = f32x4{S0[4 * g], S0[4 * g + 1], S0[4 * g + 2], S0[4 * g + 3]};
      sp[((bw * 2 + 1) * 4 + g) * 64 + lane] = f32x4{S1[4 * g], S1[4 * g + 1], S1[4 * g + 2], S1[4 * g + 3]};
    }
    lds_arrive(bc, lane); gen += 4; lds_wait(bc, gen);
    {
      LAS f32x4* sf = (LAS f32x4*)(L + L_SF);
      const int tt = bw >> 1;
#pragma unroll
      for (int gg = 0; gg < 2; ++gg) {
        const int g = 2 * (bw & 1) + gg;
        const f32x4 a = sp[((0 * 2 + tt) * 4 + g) * 64 + lane] + sp[((1 * 2 + tt) * 4 + g) * 64 + lane];
        const f32x4 b = sp[((2 * 2 + tt) * 4 + g) * 64 + lane] + sp[((3 * 2 + tt) * 4 + g) * 64 + lane];
        sf[(tt * 4 + g) * 64 + lane] = a + b;
      }
    }
    lds_arrive(bc, lane); gen += 4; lds_wait(bc, gen);
    float m = -INFINITY;
    {
      const LAS f32x4* sf = (const LAS f32x4*)(L + L_SF);
#pragma unroll
      for (int g = 0; g < 4; ++g) {
        const f32x4 a = sf[(0 * 4 + g) * 64 + lane], b = sf[(1 * 4 + g) * 64 + lane];
#pragma unroll
        for (int k = 0; k < 4; ++k) {
          const float va = rbf(rbf(a[k]) * SCALE), vb = rbf(rbf(b[k]) * SCALE);
          S0[4 * g + k] = va; S1[4 * g + k] = vb; m = fmaxf(m, fmaxf(va, vb));
        }
      }
    }
    m = fmaxf(m, __shfl_xor(m, 32));
    float l = 0.f;
#pragma unroll
    for (int i = 0; i < 16; ++i) {
      const float pa = ex2((S0[i] - m) * LOG2E), pb = ex2((S1[i] - m) * LOG2E);
      S0[i] = pa; S1[i] = pb; l += pa + pb;
    }
    l += __shfl_xor(l, 32);
    const u32x4 p00 = pack8(S0, 0), p01 = pack8(S0, 1), p10 = pack8(S1, 0), p11 = pack8(S1, 1);
    u32x4 idlo, idhi;
#pragma unroll
    for (int k = 0; k < 4; ++k) {
      const int k0 = 8 * hh + 2 * k, k1 = k0 + 1;
      idlo[k] = (cl == k0 ? 0x3f80u : 0u) | (cl == k1 ? 0x3f800000u : 0u);
      idhi[k] = (cl == k0 + 16 ? 0x3f80u : 0u) | (cl == k1 + 16 ? 0x3f800000u : 0u);
    }
    const int booff = (int)W_BO + ((sb * NH + cl) * DC + 192 * bw + 4 * hh) * 4;
#pragma unroll
    for (int ct = 0; ct < 6; ++ct) {
      f32x16 T0 = MFMA32(cf[0][2 * ct], idlo, zero);
      T0 = MFMA32(cf[0][2 * ct + 1], idhi, T0);
      f32x16 T1 = MFMA32(cf[1][2 * ct], idlo, zero);
      T1 = MFMA32(cf[1][2 * ct + 1], idhi, T1);
      f32x16 O = MFMA32(pack8(T0, 0), p00, zero);
      O = MFMA32(pack8(T0, 1), p01, O);
      O = MFMA32(pack8(T1, 0), p10, O);
      O = MFMA32(pack8(T1, 1), p11, O);
#pragma unroll
      for (int g = 0; g < 4; ++g) {
        const f32x4 v = {O[4 * g], O[4 * g + 1], O[4 * g + 2], O[4 * g + 3]};
        __builtin_amdgcn_raw_buffer_store_b128(__builtin_bit_cast(u32x4, v), rs, booff + (32 * ct + 8 * g) * 4, 0, 16);
      }
    }
    if (bw == 0 && lane < 32) {
      const u32x2 ml = {__builtin_bit_cast(unsigned, m), __builtin_bit_cast(unsigned, l)};
      __builtin_amdgcn_raw_buffer_store_b64(ml, rs, (int)W_BML + ((cl * NSB + sb) * 2) * 4, 0, 16);
    }
    VM0();
    lds_arrive(bc, lane); gen += 4; lds_wait(bc, gen);
    if (bw == 0 && lane == 0) __hip_atomic_store((GAS unsigned*)(ws + Z_FBATT) + sb, 1u, RLX_AGENT);
  }

  WoFrag wof;
  {
    u32x4 va[6]; u32x2 vb[6];
#pragma unroll
    for (int i = 0; i < 6; ++i) {
      const u16* row = wuv + ((size_t)(96 * j + 24 * bw + 4 * i + r) * NH + h) * DH;
      va[i] = ntload<u32x4>(row + 8 * c); vb[i] = ntload<u32x2>(row + 128 + 4 * c);
    }
    wo_prefetch(wof, woB, h, j, bw, lane);
    __builtin_amdgcn_sched_barrier(0);
    wait_flags(rs, ws, (int)Z_FBATT, NSB, lane);
    const u32x4 st = __builtin_amdgcn_raw_buffer_load_b128(rs, (int)W_BML + (h * NSB * 2 + lane * 4) * 4, 0, 16);
    const int q4 = lane % 24, s2 = lane / 24;
    f32x4 pv[16];
#pragma unroll
    for (int i = 0; i < 16; ++i) {
      const int s = 32 * bw + 2 * i + (s2 & 1);
      pv[i] = __builtin_bit_cast(f32x4, __builtin_amdgcn_raw_buffer_load_b128(rs, (int)W_BO + ((s * NH + h) * DC + 96 * j + 4 * q4) * 4, 0, 16));
    }
    const float m0 = asf(st[0]), l0 = asf(st[1]);
    const float m1 = asf(st[2]), l1 = asf(st[3]);
    float M = fmaxf(m0, m1);
#pragma unroll
    for (int o = 1; o < 64; o <<= 1) M = fmaxf(M, __shfl_xor(M, o));
    const float f0 = ex2((m0 - M) * LOG2E), f1 = ex2((m1 - M) * LOG2E);
    float Ls = f0 * l0 + f1 * l1;
#pragma unroll
    for (int o = 1; o < 64; o <<= 1) Ls += __shfl_xor(Ls, o);
    const float inv = 1.f / Ls;
    const float w0 = f0 * inv, w1 = f1 * inv;
    f32x4 a4 = {0.f, 0.f, 0.f, 0.f};
#pragma unroll
    for (int i = 0; i < 16; ++i) {
      const float wa_ = __shfl(w0, 16 * bw + i), wb_ = __shfl(w1, 16 * bw + i);
      a4 += ((s2 & 1) ? wb_ : wa_) * pv[i];
    }
    LAS float* cp = (LAS float*)(L + L_CP);
#pragma unroll
    for (int k = 0; k < 4; ++k) {
      const float o = __shfl(a4[k], (lane + 24) & 63);
      if (lane < 24) cp[bw * 96 + q4 * 4 + k] = a4[k] + o;
    }
    lds_arrive(bc, lane); gen += 4; lds_wait(bc, gen);
    float acc[12];
#pragma unroll
    for (int k = 0; k < 12; ++k) acc[k] = 0.f;
#pragma unroll
    for (int i = 0; i < 6; ++i) {
      const int cc = 24 * bw + 4 * i + r;
      const float p = (cp[cc] + cp[96 + cc]) + (cp[192 + cc] + cp[288 + cc]);
      fma_row(acc, p, va[i], vb[i]);
    }
    rows_to_lds(acc, (LAS float*)(L + L_RED) + bw * DH, lane);
    lds_arrive(bc, lane); gen += 4; lds_wait(bc, gen);
    const LAS float* red = (const LAS float*)(L + L_RED);
    if (t < DH) st_granule(ws, Z_GCTX + (((size_t)h * 8 + j) * DH + t) * 8, (red[t] + red[DH + t]) + (red[2 * DH + t] + red[3 * DH + t]));
    if (bw < 3) {
      float v[8];
      gather<8>(ws, Z_GCTX + ((size_t)h * 8 * DH + t) * 8, DH * 8, true, v);
      ((LAS float*)(L + L_VEC))[t] = rbf(((v[0] + v[1]) + (v[2] + v[3])) + ((v[4] + v[5]) + (v[6] + v[7])));
    }
    lds_arrive(bc, lane); gen += 4; lds_wait(bc, gen);
  }
  wo_step(wof, L, ws, out, 1, 16, h, j, bw, lane, gen);

  wo_prefetch(wof, woA, h, j, bw, lane);
  __builtin_amdgcn_sched_barrier(0);
  lds_wait((LAS unsigned*)(L + L_CTL) + 1, 4u);
  {
    const LAS float* rec = (const LAS float*)(L + L_AREC);
    const float m0 = rec[0], m1 = rec[200], m2 = rec[400], m3 = rec[600];
    const float MM = fmaxf(fmaxf(m0, m1), fmaxf(m2, m3));
    const float f0 = ex2((m0 - MM) * LOG2E), f1 = ex2((m1 - MM) * LOG2E), f2 = ex2((m2 - MM) * LOG2E), f3 = ex2((m3 - MM) * LOG2E);
    const size_t ga = Z_GAP + ((size_t)h * 8 + j) * 200 * 8;
    if (t < DH) st_granule(ws, ga + (size_t)(8 + t) * 8, (f0 * rec[8 + t] + f1 * rec[208 + t]) + (f2 * rec[408 + t] + f3 * rec[608 + t]));
    if (t == DH) st_granule(ws, ga, MM);
    if (t == DH + 1) st_granule(ws, ga + 8, (f0 * rec[1] + f1 * rec[201]) + (f2 * rec[401] + f3 * rec[601]));
    if (bw < 3) {
      float v[8], mm[8], ll[8];
      const size_t gh = Z_GAP + (size_t)h * 8 * 200 * 8;
      gather<8>(ws, gh + (size_t)(8 + t) * 8, 200 * 8, true, v);
      gather<8>(ws, gh, 200 * 8, true, mm);
      gather<8>(ws, gh + 8, 200 * 8, true, ll);
      float Mx = mm[0];
#pragma unroll
      for (int k = 1; k < 8; ++k) Mx = fmaxf(Mx, mm[k]);
      float Lsum = 0.f, o = 0.f;
#pragma unroll
      for (int k = 0; k < 8; ++k) { const float f = ex2((mm[k] - Mx) * LOG2E); Lsum = fmaf(f, ll[k], Lsum); o = fmaf(f, v[k], o); }
      ((LAS float*)(L + L_VEC))[t] = rbf(o / Lsum);
    }
    lds_arrive(bc, lane); gen += 4; lds_wait(bc, gen);
  }
  wo_step(wof, L, ws, out, 0, 0, h, j, bw, lane, gen);
}

__global__ void __launch_bounds__(512, 2) mega(const u16* __restrict__ xA, const u16* __restrict__ K, const u16* __restrict__ V,
                                               const u16* __restrict__ wqA, const u16* __restrict__ woA,
                                               const u16* __restrict__ xB, const u16* __restrict__ ckv, const u16* __restrict__ wqB,
                                               const u16* __restrict__ wuk, const u16* __restrict__ wuv, const u16* __restrict__ woB,
                                               unsigned char* ws, u16* __restrict__ out) {
  __shared__ __attribute__((aligned(16))) unsigned char smem[L_BYTES];
  LAS unsigned char* L = (LAS unsigned char*)smem;
  const int tid = threadIdx.x, lane = tid & 63;
  const int wave = __builtin_amdgcn_readfirstlane(tid >> 6);
  const int b = blockIdx.x;
  const int h = (b & 7) * 4 + (b >> 6), j = (b >> 3) & 7;
  if (tid < 16) ((LAS unsigned*)(L + L_CTL))[tid] = 0u;
  __syncthreads();
  if (wave < 4) a_role(K, V, L, h, j, wave, lane);
  else b_role(xA, wqA, woA, xB, ckv, wqB, wuk, wuv, woB, ws, out, L, h, j, wave - 4, lane);
}

extern "C" void kernel_launch(void* const* d_in, const int* in_sizes, int n_in,
                              void* d_out, int out_size, void* d_ws, size_t ws_size,
                              hipStream_t stream) {
  (void)hipMemsetAsync(d_ws, 0, Z_END, stream);
  mega<<<256, 512, 0, stream>>>((const u16*)d_in[0], (const u16*)d_in[1], (const u16*)d_in[2], (const u16*)d_in[3], (const u16*)d_in[4],
                                (const u16*)d_in[5], (const u16*)d_in[6], (const u16*)d_in[7], (const u16*)d_in[8], (const u16*)d_in[9],
                                (const u16*)d_in[10], (unsigned char*)d_ws, (u16*)d_out);
}
```

```cpp
#include <hip/hip_runtime.h>
#include <hip/hip_bf16.h>
#include <stdint.h>

typedef unsigned u32x4 __attribute__((ext_vector_type(4)));
typedef unsigned u32x2 __attribute__((ext_vector_type(2)));
typedef __bf16 bf16x2 __attribute__((ext_vector_type(2)));
typedef __bf16 bf16x8 __attribute__((ext_vector_type(8)));
typedef float f32x16 __attribute__((ext_vector_type(16)));
typedef float f32x4 __attribute__((ext_vector_type(4)));
typedef unsigned short u16;
typedef unsigned long long u64;

#define DI __device__ __forceinline__
#define GAS __attribute__((address_space(1)))
#define LAS __attribute__((address_space(3)))
#define RLX_AGENT __ATOMIC_RELAXED, __HIP_MEMORY_SCOPE_AGENT

constexpr int NH = 32, SK = 8192, DH = 192, DM = 640, DC = 768, HD = NH * DH;
constexpr float SCALE = 0.07216878364870322f;
constexpr float LOG2E = 1.4426950408889634f;
#define A_KR 2
#define A_NB 1
constexpr int NA = 6;
constexpr int NSB = 64;
constexpr unsigned kSpinCap = 1u << 18;

constexpr size_t Z_TMO = 0;
constexpr size_t Z_FQABS = 1024;
constexpr size_t Z_FBATT = 2048;
constexpr size_t Z_GQP = 4096;
constexpr size_t Z_GCTX = Z_GQP + (size_t)NH * 8 * 384 * 8;
constexpr size_t Z_GAP = Z_GCTX + (size_t)NH * 2 * 192 * 8;
constexpr size_t Z_GOUT = Z_GAP + (size_t)NH * NA * 200 * 8;
constexpr size_t Z_END = Z_GOUT + (size_t)2 * NH * DM * 8;
constexpr size_t W_QH = Z_END;
constexpr size_t W_QL = W_QH + (size_t)NH * DC * 2;
constexpr size_t W_BML = W_QL + (size_t)NH * DC * 2;
constexpr size_t W_BO = W_BML + (size_t)NH * NSB * 2 * 4;
constexpr size_t W_END = W_BO + (size_t)NSB * NH * DC * 4;
static_assert(Z_END % 16 == 0 && W_END < (1ull << 31), "ws layout");

constexpr int L_QS = 0;
constexpr int L_VEC = 512;
constexpr int L_RED = 2048;
constexpr int L_BIG = 16384;
constexpr int L_SF = L_BIG + 131072;
constexpr int L_BYTES = L_SF + 16384;

DI float dot2(unsigned a, unsigned b, float c) {
  return __builtin_amdgcn_fdot2_f32_bf16(__builtin_bit_cast(bf16x2, a), __builtin_bit_cast(bf16x2, b), c, false);
}
DI float rbf(float x) { return (float)(__bf16)x; }
DI float asf(unsigned v) { return __builtin_bit_cast(float, v); }
DI unsigned asu(float v) { return __builtin_bit_cast(unsigned, v); }
DI float bflo(unsigned v) { return __builtin_bit_cast(float, v << 16); }
DI float bfhi(unsigned v) { return __builtin_bit_cast(float, v & 0xffff0000u); }
DI u16 f2bf(float x) { return __builtin_bit_cast(u16, (__bf16)x); }
DI unsigned pack2(float lo, float hi) { return (unsigned)f2bf(lo) | ((unsigned)f2bf(hi) << 16); }
template <int CTRL> DI float dpp(float x) {
  return __builtin_bit_cast(float, __builtin_amdgcn_mov_dpp(__builtin_bit_cast(int, x), CTRL, 0xf, 0xf, true));
}
DI float row_sum16(float x) {
  x += dpp<0x128>(x); x += dpp<0x124>(x); x += dpp<0x122>(x); x += dpp<0x121>(x); return x;
}
DI float ex2(float x) { return __builtin_amdgcn_exp2f(x); }
template <typename T> DI T ntload(const void* p) { return __builtin_nontemporal_load(reinterpret_cast<const T*>(p)); }
#define MFMA32(a, b, c) __builtin_amdgcn_mfma_f32_32x32x16_bf16(__builtin_bit_cast(bf16x8, a), __builtin_bit_cast(bf16x8, b), (c), 0, 0, 0)
DI u32x4 pack8(const f32x16& x, int s) {
  u32x4 p;
#pragma unroll
  for (int j = 0; j < 4; ++j) p[j] = pack2(x[8 * s + 2 * j], x[8 * s + 2 * j + 1]);
  return p;
}
DI int opaque(int x) { asm volatile("" : "+v"(x)); return x; }
#define VM0() asm volatile("s_waitcnt vmcnt(0)" ::: "memory")
#define CBAR() asm volatile("" ::: "memory")
#define WGBAR() do { asm volatile("s_waitcnt lgkmcnt(0)" ::: "memory"); __builtin_amdgcn_s_barrier(); asm volatile("" ::: "memory"); } while (0)

#define STAMP(k) do { } while (0)

DI void st_granule(unsigned char* ws, size_t off, float v) {
  __hip_atomic_store((GAS u64*)(ws + off), (1ull << 32) | (u64)asu(v), RLX_AGENT);
}
DI u64 ld_granule(unsigned char* ws, size_t off) { return __hip_atomic_load((GAS u64*)(ws + off), RLX_AGENT); }
DI bool spin_fail(unsigned& spins, unsigned char* ws) {
  __builtin_amdgcn_s_sleep(1);
  ++spins;
  if (spins > kSpinCap) { __hip_atomic_store((GAS unsigned*)(ws + Z_TMO), 1u, RLX_AGENT); return true; }
  if ((spins & 255u) == 0u) return __builtin_amdgcn_readfirstlane(__hip_atomic_load((GAS unsigned*)(ws + Z_TMO), RLX_AGENT)) != 0u;
  return false;
}
template <int N> DI void gather(unsigned char* ws, size_t off, size_t stride, bool active, float (&v)[N]) {
  for (unsigned spins = 0;;) {
    bool ok = true;
    if (active) {
#pragma unroll
      for (int k = 0; k < N; ++k) {
        const u64 x = ld_granule(ws, off + k * stride);
        v[k] = asf((unsigned)x);
        ok &= (x >> 32) == 1ull;
      }
    }
    if (__all(ok)) break;
    if (spin_fail(spins, ws)) break;
  }
  CBAR();
}
DI void wait_flags64(unsigned char* ws, size_t off, int lane) {
  for (unsigned spins = 0;;) {
    const unsigned f = __hip_atomic_load((GAS unsigned*)(ws + off) + lane, RLX_AGENT);
    if (__all(f != 0u)) break;
    if (spin_fail(spins, ws)) break;
  }
  CBAR();
}

DI void fma_row(float (&acc)[12], float p, const u32x4& a, const u32x2& b) {
#pragma unroll
  for (int k = 0; k < 4; ++k) { acc[2 * k] = fmaf(p, bflo(a[k]), acc[2 * k]); acc[2 * k + 1] = fmaf(p, bfhi(a[k]), acc[2 * k + 1]); }
#pragma unroll
  for (int k = 0; k < 2; ++k) { acc[8 + 2 * k] = fmaf(p, bflo(b[k]), acc[8 + 2 * k]); acc[8 + 2 * k + 1] = fmaf(p, bfhi(b[k]), acc[8 + 2 * k + 1]); }
}
DI void rows_to_lds(float (&acc)[12], LAS float* dst, int lane) {
  const int r = lane >> 4, c = lane & 15;
#pragma unroll
  for (int k = 0; k < 12; ++k) { float a = acc[k]; a += __shfl_xor(a, 16); a += __shfl_xor(a, 32); acc[k] = a; }
  if (r == 0) {
#pragma unroll
    for (int k = 0; k < 8; ++k) dst[8 * c + k] = acc[k];
#pragma unroll
    for (int k = 0; k < 4; ++k) dst[128 + 4 * c + k] = acc[8 + k];
  }
}

DI void out_reduce(unsigned char* ws, u16* __restrict__ out, LAS unsigned char* L, int mat, int col0, int tid) {
  const int col = tid & 127, qd = tid >> 7;
  float v[8];
  gather<8>(ws, Z_GOUT + (((size_t)mat * NH + qd * 8) * DM + col0 + col) * 8, (size_t)DM * 8, true, v);
  LAS float* red = (LAS float*)(L + L_RED);
  red[tid] = ((v[0] + v[1]) + (v[2] + v[3])) + ((v[4] + v[5]) + (v[6] + v[7]));
  WGBAR();
  if (tid < 128) out[mat * DM + col0 + tid] = f2bf((red[tid] + red[128 + tid]) + (red[256 + tid] + red[384 + tid]));
}

struct QLoads { u32x4 a[5]; u32x2 b[5]; float x[5]; };
DI void q_issue(QLoads& Q, const u16* __restrict__ xA, const u16* __restrict__ wqA, const u16* __restrict__ xB,
                const u16* __restrict__ wqB, int h, int jj, int wave, int lane) {
  const int r = lane >> 4, c = lane & 15;
  const u16* x = wave < 4 ? xA : xB;
  const u16* w = wave < 4 ? wqA : wqB;
#pragma unroll
  for (int i = 0; i < 5; ++i) {
    const int row = 80 * jj + 20 * (wave & 3) + 4 * i + r;
    const u16* p = w + (size_t)row * HD + h * DH;
    Q.a[i] = ntload<u32x4>(p + 8 * c); Q.b[i] = ntload<u32x2>(p + 128 + 4 * c);
    Q.x[i] = bflo((unsigned)x[row]);
  }
}
DI void q_partial(QLoads& Q, LAS unsigned char* L, int wave, int lane) {
  float acc[12];
#pragma unroll
  for (int k = 0; k < 12; ++k) acc[k] = 0.f;
#pragma unroll
  for (int i = 0; i < 5; ++i) fma_row(acc, Q.x[i], Q.a[i], Q.b[i]);
  rows_to_lds(acc, (LAS float*)(L + L_RED) + wave * DH, lane);
}
DI void q_exchange(unsigned char* ws, LAS unsigned char* L, int h, int jj, int part, int wave, int lane) {
  const int tid = wave * 64 + lane;
  const LAS float* red = (const LAS float*)(L + L_RED);
  if (tid < 384) {
    const int mt = tid >= DH ? 1 : 0, d = tid - mt * DH;
    const LAS float* rp = red + mt * 4 * DH + d;
    st_granule(ws, Z_GQP + (((size_t)h * 8 + jj) * 384 + tid) * 8, (rp[0] + rp[DH]) + (rp[2 * DH] + rp[3 * DH]));
  }
  if (wave < 3) {
    float v[8];
    gather<8>(ws, Z_GQP + ((size_t)h * 8 * 384 + part * DH + tid) * 8, 384 * 8, true, v);
    ((LAS u16*)(L + L_QS))[tid] = f2bf(((v[0] + v[1]) + (v[2] + v[3])) + ((v[4] + v[5]) + (v[6] + v[7])));
  }
}

DI void a_role(const u16* __restrict__ xA, const u16* __restrict__ wqA, const u16* __restrict__ xB, const u16* __restrict__ wqB,
               const u16* __restrict__ K, const u16* __restrict__ V, const u16* __restrict__ woA,
               unsigned char* ws, u16* __restrict__ out, LAS unsigned char* L, int h, int j, int wave, int lane) {
  const int r = lane >> 4, c = lane & 15, tid = wave * 64 + lane;
  STAMP(0);
  QLoads Q;
  q_issue(Q, xA, wqA, xB, wqB, h, j, wave, lane);
  constexpr int KR = A_KR, NB = A_NB, TK = 4 * KR, NTH = SK / TK;
  const int gw = j * 8 + wave;
  const int tb = (NTH * gw) / 48, te = (NTH * (gw + 1)) / 48, NT = te - tb;
  const size_t base = ((size_t)h * SK + tb * TK + r) * DH;
  const u16* kp = K + base;
  const u16* vp = V + base;
  u32x4 ka[NB][KR], va[NB][KR]; u32x2 kb[NB][KR], vb[NB][KR];
#define LOADT(buf, t) do { _Pragma("unroll") for (int i = 0; i < KR; ++i) { \
    const u16* kr = kp + (size_t)((t) * TK + i * 4) * DH; const u16* vr = vp + (size_t)((t) * TK + i * 4) * DH; \
    ka[buf][i] = ntload<u32x4>(kr + 8 * c); kb[buf][i] = ntload<u32x2>(kr + 128 + 4 * c); \
    va[buf][i] = ntload<u32x4>(vr + 8 * c); vb[buf][i] = ntload<u32x2>(vr + 128 + 4 * c); } } while (0)
  q_partial(Q, L, wave, lane);
  STAMP(10);
  WGBAR();
  STAMP(11);
  q_exchange(ws, L, h, j, 0, wave, lane);
  STAMP(12);
  __builtin_amdgcn_sched_barrier(0);
  WGBAR();
#pragma unroll
  for (int b = 0; b < NB; ++b) LOADT(b, b);
  __builtin_amdgcn_sched_barrier(0);
  STAMP(1);
  const u32x4 qa = *(const LAS u32x4*)(L + L_QS + 16 * c);
  const u32x2 qb = *(const LAS u32x2*)(L + L_QS + 256 + 8 * c);
  float m = -INFINITY, l = 0.f, acc[12];
#pragma unroll
  for (int k = 0; k < 12; ++k) acc[k] = 0.f;
  for (int t0 = 0; t0 < NT; t0 += NB) {
#pragma unroll
    for (int b = 0; b < NB; ++b) {
      const int t = t0 + b;
      if (t < NT) {
        float s[KR];
        float mn = m;
#pragma unroll
        for (int i = 0; i < KR; ++i) {
          float d = 0.f;
#pragma unroll
          for (int k = 0; k < 4; ++k) d = dot2(ka[b][i][k], qa[k], d);
          d = dot2(kb[b][i][0], qb[0], d); d = dot2(kb[b][i][1], qb[1], d);
          d = row_sum16(d);
          s[i] = rbf(rbf(d) * SCALE);
          mn = fmaxf(mn, s[i]);
        }
        const float alpha = ex2((m - mn) * LOG2E);
        m = mn;
        float ps = 0.f;
#pragma unroll
        for (int i = 0; i < KR; ++i) { s[i] = ex2((s[i] - mn) * LOG2E); ps += s[i]; }
        l = l * alpha + ps;
#pragma unroll
        for (int k = 0; k < 12; ++k) acc[k] *= alpha;
#pragma unroll
        for (int i = 0; i < KR; ++i) fma_row(acc, s[i], va[b][i], vb[b][i]);
        if (t + NB < NT) LOADT(b, t + NB);
      }
    }
  }
#undef LOADT
  STAMP(2);
  u32x4 wv[6];
  if (j < 5) {
#pragma unroll
    for (int i = 0; i < 6; ++i) wv[i] = ntload<u32x4>(woA + (size_t)(h * DH + 24 * wave + 4 * i + r) * DM + 128 * j + 8 * c);
  }
  float M = fmaxf(m, __shfl_xor(m, 16)); M = fmaxf(M, __shfl_xor(M, 32));
  const float f = ex2((m - M) * LOG2E);
  l *= f; l += __shfl_xor(l, 16); l += __shfl_xor(l, 32);
#pragma unroll
  for (int k = 0; k < 12; ++k) acc[k] *= f;
  LAS float* red = (LAS float*)(L + L_RED);
  rows_to_lds(acc, red + wave * 200 + 8, lane);
  if (lane == 0) { red[wave * 200] = M; red[wave * 200 + 1] = l; }
  WGBAR();
  {
    float mw[8], MM = -INFINITY;
#pragma unroll
    for (int w = 0; w < 8; ++w) { mw[w] = red[w * 200]; MM = fmaxf(MM, mw[w]); }
    const int idx = tid < DH ? 8 + tid : 1;
    float s = 0.f;
#pragma unroll
    for (int w = 0; w < 8; ++w) s = fmaf(ex2((mw[w] - MM) * LOG2E), red[w * 200 + idx], s);
    const size_t ga = Z_GAP + ((size_t)h * NA + j) * 200 * 8;
    if (tid < DH) st_granule(ws, ga + (size_t)(8 + tid) * 8, s);
    if (tid == DH) st_granule(ws, ga + 8, s);
    if (tid == DH + 1) st_granule(ws, ga, MM);
  }
  if (j < 5) {
    if (wave < 3) {
      float v[NA], mm[NA], ll[NA];
      const size_t gh = Z_GAP + (size_t)h * NA * 200 * 8;
      gather<NA>(ws, gh + (size_t)(8 + tid) * 8, 200 * 8, true, v);
      gather<NA>(ws, gh, 200 * 8, true, mm);
      gather<NA>(ws, gh + 8, 200 * 8, true, ll);
      float Mx = mm[0];
#pragma unroll
      for (int k = 1; k < NA; ++k) Mx = fmaxf(Mx, mm[k]);
      float Lsum = 0.f, o = 0.f;
#pragma unroll
      for (int k = 0; k < NA; ++k) { const float g = ex2((mm[k] - Mx) * LOG2E); Lsum = fmaf(g, ll[k], Lsum); o = fmaf(g, v[k], o); }
      ((LAS float*)(L + L_VEC))[tid] = rbf(o / Lsum);
    }
    WGBAR();
    STAMP(3);
    const LAS float* vec = (const LAS float*)(L + L_VEC);
    float a8[8];
#pragma unroll
    for (int k = 0; k < 8; ++k) a8[k] = 0.f;
#pragma unroll
    for (int i = 0; i < 6; ++i) {
      const float x = vec[24 * wave + 4 * i + r];
#pragma unroll
      for (int k = 0; k < 4; ++k) { a8[2 * k] = fmaf(x, bflo(wv[i][k]), a8[2 * k]); a8[2 * k + 1] = fmaf(x, bfhi(wv[i][k]), a8[2 * k + 1]); }
    }
#pragma unroll
    for (int k = 0; k < 8; ++k) { a8[k] += __shfl_xor(a8[k], 16); a8[k] += __shfl_xor(a8[k], 32); }
    LAS float* r2 = (LAS float*)(L + L_BIG);
    if (r == 0) {
#pragma unroll
      for (int k = 0; k < 8; ++k) r2[wave * 128 + 8 * c + k] = a8[k];
    }
    WGBAR();
    if (tid < 128) {
      float s = 0.f;
#pragma unroll
      for (int w = 0; w < 8; ++w) s += r2[w * 128 + tid];
      st_granule(ws, Z_GOUT + ((size_t)h * DM + 128 * j + tid) * 8, s);
    }
    STAMP(4);
    if (h == 4 * j + 1) out_reduce(ws, out, L, 0, 128 * j, tid);
    STAMP(5);
  }
}

DI void b_role(const u16* __restrict__ xA, const u16* __restrict__ wqA, const u16* __restrict__ xB, const u16* __restrict__ wqB,
               const u16* __restrict__ ckv, const u16* __restrict__ wuk, const u16* __restrict__ wuv, const u16* __restrict__ woB,
               unsigned char* ws, u16* __restrict__ out, LAS unsigned char* L, int h, int half, int wave, int lane) {
  const int r = lane >> 4, c = lane & 15, tid = wave * 64 + lane;
  const int cl = lane & 31, hh = lane >> 5;
  const int kb_ = 2 * h + half;
  const __amdgpu_buffer_rsrc_t rs = __builtin_amdgcn_make_buffer_rsrc((void*)ws, 0, (int)W_END, 0x00020000);
  STAMP(0);
  u32x4 cf[4][6];
  {
    QLoads Q;
    q_issue(Q, xA, wqA, xB, wqB, h, NA + half, wave, lane);
    u32x4 ua[12]; u32x2 ub[12];
#pragma unroll
    for (int i = 0; i < 12; ++i) {
      const u16* row = wuk + ((size_t)(384 * half + 48 * wave + 4 * i + r) * NH + h) * DH;
      ua[i] = ntload<u32x4>(row + 8 * c); ub[i] = ntload<u32x2>(row + 128 + 4 * c);
    }
#define LOADC() do { _Pragma("unroll") for (int t = 0; t < 4; ++t) _Pragma("unroll") for (int s = 0; s < 6; ++s) \
        cf[t][s] = *reinterpret_cast<const u32x4*>(ckv + (size_t)(128 * kb_ + 32 * t + cl) * DC + 96 * wave + 16 * s + 8 * hh); } while (0)
    __builtin_amdgcn_sched_barrier(0);
    q_partial(Q, L, wave, lane);
    __builtin_amdgcn_sched_barrier(0);
    WGBAR();
    q_exchange(ws, L, h, NA + half, 1, wave, lane);
    __builtin_amdgcn_sched_barrier(0);
    LOADC();
    __builtin_amdgcn_sched_barrier(0);
    WGBAR();
#undef LOADC
    STAMP(1);
    const u32x4 qa = *(const LAS u32x4*)(L + L_QS + 16 * c);
    const u32x2 qb = *(const LAS u32x2*)(L + L_QS + 256 + 8 * c);
    LAS float* qv = (LAS float*)(L + L_VEC);
#pragma unroll
    for (int i = 0; i < 12; ++i) {
      float s = 0.f;
#pragma unroll
      for (int k = 0; k < 4; ++k) s = dot2(ua[i][k], qa[k], s);
      s = dot2(ub[i][0], qb[0], s); s = dot2(ub[i][1], qb[1], s);
      s = row_sum16(s);
      if (c == 0) qv[48 * wave + 4 * i + r] = s;
    }
    WGBAR();
    if (wave < 2) {
      if (lane < 48) {
        u32x4 o;
#pragma unroll
        for (int k = 0; k < 4; ++k) {
          const float a = qv[8 * lane + 2 * k], b = qv[8 * lane + 2 * k + 1];
          const float ah = rbf(a), bh = rbf(b);
          o[k] = wave ? pack2(a - ah, b - bh) : pack2(ah, bh);
        }
        __builtin_amdgcn_raw_buffer_store_b128(o, rs, (int)((wave ? W_QL : W_QH) + ((size_t)h * DC + 384 * half + 8 * lane) * 2), 0, 16);
      }
      VM0();
    }
    WGBAR();
    if (tid == 0) __hip_atomic_store((GAS unsigned*)(ws + Z_FQABS) + kb_, 1u, RLX_AGENT);
  }
  STAMP(2);
  {
    wait_flags64(ws, Z_FQABS, lane);
    STAMP(3);
    u32x4 qh[6], ql[6];
    const int qoff = (cl * DC + 96 * wave + 8 * hh) * 2;
#pragma unroll
    for (int s = 0; s < 6; ++s) {
      qh[s] = __builtin_amdgcn_raw_buffer_load_b128(rs, (int)W_QH + qoff + s * 32, 0, 16);
      ql[s] = __builtin_amdgcn_raw_buffer_load_b128(rs, (int)W_QL + qoff + s * 32, 0, 16);
    }
    __builtin_amdgcn_sched_barrier(0);
    f32x16 zero;
#pragma unroll
    for (int i = 0; i < 16; ++i) zero[i] = 0.f;
    LAS f32x4* sp = (LAS f32x4*)(L + L_BIG);
#pragma unroll
    for (int t = 0; t < 4; ++t) {
      f32x16 S = zero;
#pragma unroll
      for (int s = 0; s < 6; ++s) S = MFMA32(cf[t][s], ql[s], S);
#pragma unroll
      for (int s = 0; s < 6; ++s) S = MFMA32(cf[t][s], qh[s], S);
#pragma unroll
      for (int g = 0; g < 4; ++g) sp[((wave * 4 + t) * 4 + g) * 64 + lane] = f32x4{S[4 * g], S[4 * g + 1], S[4 * g + 2], S[4 * g + 3]};
    }
    STAMP(4);
    WGBAR();
    LAS f32x4* sf = (LAS f32x4*)(L + L_SF);
    {
      const int t = wave >> 1;
#pragma unroll
      for (int gg = 0; gg < 2; ++gg) {
        const int g = 2 * (wave & 1) + gg;
        f32x4 a = sp[((0 * 4 + t) * 4 + g) * 64 + lane];
#pragma unroll
        for (int w = 1; w < 8; ++w) a += sp[((w * 4 + t) * 4 + g) * 64 + lane];
        sf[(t * 4 + g) * 64 + lane] = a;
      }
    }
    WGBAR();
    f32x16 P[4];
    float m = -INFINITY;
#pragma unroll
    for (int t = 0; t < 4; ++t)
#pragma unroll
      for (int g = 0; g < 4; ++g) {
        const f32x4 a = sf[(t * 4 + g) * 64 + lane];
#pragma unroll
        for (int k = 0; k < 4; ++k) { const float v = rbf(rbf(a[k]) * SCALE); P[t][4 * g + k] = v; m = fmaxf(m, v); }
      }
    m = fmaxf(m, __shfl_xor(m, 32));
    float l = 0.f;
#pragma unroll
    for (int t = 0; t < 4; ++t)
#pragma unroll
      for (int i = 0; i < 16; ++i) { const float p = ex2((P[t][i] - m) * LOG2E); P[t][i] = p; l += p; }
    l += __shfl_xor(l, 32);
    u32x4 pb[4][2];
#pragma unroll
    for (int t = 0; t < 4; ++t) { pb[t][0] = pack8(P[t], 0); pb[t][1] = pack8(P[t], 1); }
    u32x4 idlo, idhi;
#pragma unroll
    for (int k = 0; k < 4; ++k) {
      const int k0 = 8 * hh + 2 * k, k1 = k0 + 1;
      idlo[k] = (cl == k0 ? 0x3f80u : 0u) | (cl == k1 ? 0x3f800000u : 0u);
      idhi[k] = (cl == k0 + 16 ? 0x3f80u : 0u) | (cl == k1 + 16 ? 0x3f800000u : 0u);
    }
    STAMP(5);
    LAS float* ost = (LAS float*)(L + L_BIG) + wave * 3200;
#pragma unroll
    for (int ct = 0; ct < 3; ++ct) {
      f32x16 O = zero;
#pragma unroll
      for (int t = 0; t < 4; ++t) {
        f32x16 T = MFMA32(cf[t][2 * ct], idlo, zero);
        T = MFMA32(cf[t][2 * ct + 1], idhi, T);
        O = MFMA32(pack8(T, 0), pb[t][0], O);
        O = MFMA32(pack8(T, 1), pb[t][1], O);
      }
#pragma unroll
      for (int g = 0; g < 4; ++g)
        *(LAS f32x4*)(ost + cl * 100 + 32 * ct + 8 * g + 4 * hh) = f32x4{O[4 * g], O[4 * g + 1], O[4 * g + 2], O[4 * g + 3]};
    }
    asm volatile("s_waitcnt lgkmcnt(0)" ::: "memory");
#pragma unroll
    for (int i = 0; i < 12; ++i) {
      const int e = i * 64 + lane, hd = e / 24, q4 = e % 24;
      const f32x4 v = *(const LAS f32x4*)(ost + hd * 100 + 4 * q4);
      __builtin_amdgcn_raw_buffer_store_b128(__builtin_bit_cast(u32x4, v), rs, (int)W_BO + ((kb_ * NH + hd) * DC + 96 * wave + 4 * q4) * 4, 0, 16);
    }
    if (wave == 0 && lane < 32) {
      const u32x2 ml = {asu(m), asu(l)};
      __builtin_amdgcn_raw_buffer_store_b64(ml, rs, (int)W_BML + ((cl * NSB + kb_) * 2) * 4, 0, 16);
    }
    STAMP(6);
    VM0();
    WGBAR();
    if (tid == 0) __hip_atomic_store((GAS unsigned*)(ws + Z_FBATT) + kb_, 1u, RLX_AGENT);
    STAMP(7);
  }
  {
    const int lane2 = opaque(lane);
    const int r = lane2 >> 4, c = lane2 & 15, tid = wave * 64 + lane2;
    u32x4 va[12]; u32x2 vb[12];
#pragma unroll
    for (int i = 0; i < 12; ++i) {
      const u16* row = wuv + ((size_t)(384 * half + 48 * wave + 4 * i + r) * NH + h) * DH;
      va[i] = ntload<u32x4>(row + 8 * c); vb[i] = ntload<u32x2>(row + 128 + 4 * c);
    }
    __builtin_amdgcn_sched_barrier(0);
    wait_flags64(ws, Z_FBATT, lane);
    STAMP(8);
    const u32x2 st = __builtin_amdgcn_raw_buffer_load_b64(rs, (int)W_BML + (h * NSB + lane2) * 8, 0, 16);
    f32x4 pv[12];
#pragma unroll
    for (int i = 0; i < 12; ++i) {
      const int e = i * 64 + lane2, s = 8 * wave + e / 96, q4 = e % 96;
      pv[i] = __builtin_bit_cast(f32x4, __builtin_amdgcn_raw_buffer_load_b128(rs, (int)W_BO + ((s * NH + h) * DC + 384 * half + 4 * q4) * 4, 0, 16));
    }
    const float m0 = asf(st[0]), l0 = asf(st[1]);
    float M = m0;
#pragma unroll
    for (int o = 1; o < 64; o <<= 1) M = fmaxf(M, __shfl_xor(M, o));
    const float f0 = ex2((m0 - M) * LOG2E);
    float Ls = f0 * l0;
#pragma unroll
    for (int o = 1; o < 64; o <<= 1) Ls += __shfl_xor(Ls, o);
    const float w0 = f0 / Ls;
    LAS f32x4* acc4 = (LAS f32x4*)(L + L_BIG);
#pragma unroll
    for (int i = 0; i < 12; ++i) {
      const int e = i * 64 + lane2, s = 8 * wave + e / 96;
      const float w = __shfl(w0, s);
      acc4[8 * wave * 96 + e] = w * pv[i];
    }
    __builtin_amdgcn_sched_barrier(0);
    u32x4 wo[24];
    const int lc = lane2 < 40 ? lane2 : 0;
#pragma unroll
    for (int i = 0; i < 24; ++i) wo[i] = ntload<u32x4>(woB + (size_t)(h * DH + 24 * wave + i) * DM + 320 * half + 8 * lc);
    __builtin_amdgcn_sched_barrier(0);
    WGBAR();
    if (tid < 384) {
      const LAS float* af = (const LAS float*)(L + L_BIG) + tid;
      float s0 = 0.f, s1 = 0.f, s2 = 0.f, s3 = 0.f;
#pragma unroll
      for (int s = 0; s < 64; s += 4) { s0 += af[s * 384]; s1 += af[(s + 1) * 384]; s2 += af[(s + 2) * 384]; s3 += af[(s + 3) * 384]; }
      ((LAS float*)(L + L_VEC))[tid] = (s0 + s1) + (s2 + s3);
    }
    WGBAR();
    STAMP(9);
    float acc[12];
#pragma unroll
    for (int k = 0; k < 12; ++k) acc[k] = 0.f;
    const LAS float* cv = (const LAS float*)(L + L_VEC);
#pragma unroll
    for (int i = 0; i < 12; ++i) fma_row(acc, cv[48 * wave + 4 * i + r], va[i], vb[i]);
    LAS float* red = (LAS float*)(L + L_RED);
    rows_to_lds(acc, red + wave * DH, lane);
    WGBAR();
    if (tid < DH) {
      float s = 0.f;
#pragma unroll
      for (int w = 0; w < 8; ++w) s += red[w * DH + tid];
      st_granule(ws, Z_GCTX + (((size_t)h * 2 + half) * DH + tid) * 8, s);
      float v[2];
      gather<2>(ws, Z_GCTX + ((size_t)h * 2 * DH + tid) * 8, DH * 8, true, v);
      ((LAS float*)(L + L_VEC))[tid] = rbf(v[0] + v[1]);
    }
    WGBAR();
    STAMP(10);
    float a8[8];
#pragma unroll
    for (int k = 0; k < 8; ++k) a8[k] = 0.f;
#pragma unroll
    for (int i = 0; i < 24; ++i) {
      const float x = cv[24 * wave + i];
#pragma unroll
      for (int k = 0; k < 4; ++k) { a8[2 * k] = fmaf(x, bflo(wo[i][k]), a8[2 * k]); a8[2 * k + 1] = fmaf(x, bfhi(wo[i][k]), a8[2 * k + 1]); }
    }
    LAS float* r2 = (LAS float*)(L + L_BIG);
    if (lane < 40) {
#pragma unroll
      for (int k = 0; k < 8; ++k) r2[wave * 320 + 8 * lane + k] = a8[k];
    }
    WGBAR();
    if (tid < 320) {
      float s = 0.f;
#pragma unroll
      for (int w = 0; w < 8; ++w) s += r2[w * 320 + tid];
      st_granule(ws, Z_GOUT + (((size_t)NH + h) * DM + 320 * half + tid) * 8, s);
    }
    STAMP(11);
    if ((kb_ % 12) == 3 && kb_ < 60) out_reduce(ws, out, L, 1, 128 * (kb_ / 12), tid);
    STAMP(12);
  }
}

__global__ void __launch_bounds__(512, 2) mega(const u16* __restrict__ xA, const u16* __restrict__ K, const u16* __restrict__ V,
                                               const u16* __restrict__ wqA, const u16* __restrict__ woA,
                                               const u16* __restrict__ xB, const u16* __restrict__ ckv, const u16* __restrict__ wqB,
                                               const u16* __restrict__ wuk, const u16* __restrict__ wuv, const u16* __restrict__ woB,
                                               unsigned char* ws, u16* __restrict__ out) {
  extern __shared__ __attribute__((aligned(16))) unsigned char smem[];
  LAS unsigned char* L = (LAS unsigned char*)smem;
  const int tid = threadIdx.x, lane = tid & 63;
  const int wave = __builtin_amdgcn_readfirstlane(tid >> 6);
  const int b = blockIdx.x;
  const int h = (b & 7) * 4 + (b >> 6), jj = (b >> 3) & 7;
  if (jj < NA) a_role(xA, wqA, xB, wqB, K, V, woA, ws, out, L, h, jj, wave, lane);
  else b_role(xA, wqA, xB, wqB, ckv, wuk, wuv, woB, ws, out, L, h, jj - NA, wave, lane);
}

extern "C" void kernel_launch(void* const* d_in, const int* in_sizes, int n_in,
                              void* d_out, int out_size, void* d_ws, size_t ws_size,
                              hipStream_t stream) {
  (void)hipFuncSetAttribute((const void*)mega, hipFuncAttributeMaxDynamicSharedMemorySize, L_BYTES);
  (void)hipMemsetAsync(d_ws, 0, Z_END, stream);
  mega<<<256, 512, L_BYTES, stream>>>((const u16*)d_in[0], (const u16*)d_in[1], (const u16*)d_in[2], (const u16*)d_in[3], (const u16*)d_in[4],
                                      (const u16*)d_in[5], (const u16*)d_in[6], (const u16*)d_in[7], (const u16*)d_in[8], (const u16*)d_in[9],
                                      (const u16*)d_in[10], (unsigned char*)d_ws, (u16*)d_out);
}
```

```cpp
#include <hip/hip_runtime.h>
#include <hip/hip_bf16.h>
#include <stdint.h>

typedef unsigned u32x4 __attribute__((ext_vector_type(4)));
typedef unsigned u32x2 __attribute__((ext_vector_type(2)));
typedef __bf16 bf16x2 __attribute__((ext_vector_type(2)));
typedef __bf16 bf16x8 __attribute__((ext_vector_type(8)));
typedef float f32x16 __attribute__((ext_vector_type(16)));
typedef float f32x4 __attribute__((ext_vector_type(4)));
typedef unsigned short u16;
typedef unsigned long long u64;

#define DI __device__ __forceinline__
#define GAS __attribute__((address_space(1)))
#define LAS __attribute__((address_space(3)))
#define RLX_AGENT __ATOMIC_RELAXED, __HIP_MEMORY_SCOPE_AGENT

constexpr int NH = 32, SK = 8192, DH = 192, DM = 640, DC = 768, HD = NH * DH;
constexpr float SCALE = 0.07216878364870322f;
constexpr float LOG2E = 1.4426950408889634f;
#define A_KR 2
#define A_NB 1
constexpr int NA = 6;
constexpr int NSB = 64;
constexpr unsigned kSpinCap = 1u << 18;

constexpr size_t Z_TMO = 0;
constexpr size_t Z_FQABS = 1024;
constexpr size_t Z_FBATT = 2048;
constexpr size_t Z_GQP = 4096;
constexpr size_t Z_GCTX = Z_GQP + (size_t)NH * 8 * 384 * 8;
constexpr size_t Z_GAP = Z_GCTX + (size_t)NH * 2 * 192 * 8;
constexpr size_t Z_GOUT = Z_GAP + (size_t)NH * NA * 200 * 8;
constexpr size_t Z_END = Z_GOUT + (size_t)2 * NH * DM * 8;
constexpr size_t W_QH = Z_END;
constexpr size_t W_QL = W_QH + (size_t)NH * DC * 2;
constexpr size_t W_BML = W_QL + (size_t)NH * DC * 2;
constexpr size_t W_BO = W_BML + (size_t)NH * NSB * 2 * 4;
constexpr size_t W_END = W_BO + (size_t)NSB * NH * DC * 4;
static_assert(Z_END % 16 == 0 && W_END < (1ull << 31), "ws layout");

constexpr int L_QS = 0;
constexpr int L_VEC = 512;
constexpr int L_RED = 2048;
constexpr int L_BIG = 16384;
constexpr int L_SF = L_BIG + 131072;
constexpr int L_BYTES = L_SF + 16384;

DI float dot2(unsigned a, unsigned b, float c) {
  return __builtin_amdgcn_fdot2_f32_bf16(__builtin_bit_cast(bf16x2, a), __builtin_bit_cast(bf16x2, b), c, false);
}
DI float rbf(float x) { return (float)(__bf16)x; }
DI float asf(unsigned v) { return __builtin_bit_cast(float, v); }
DI unsigned asu(float v) { return __builtin_bit_cast(unsigned, v); }
DI float bflo(unsigned v) { return __builtin_bit_cast(float, v << 16); }
DI float bfhi(unsigned v) { return __builtin_bit_cast(float, v & 0xffff0000u); }
DI u16 f2bf(float x) { return __builtin_bit_cast(u16, (__bf16)x); }
DI unsigned pack2(float lo, float hi) { return (unsigned)f2bf(lo) | ((unsigned)f2bf(hi) << 16); }
template <int CTRL> DI float dpp(float x) {
  return __builtin_bit_cast(float, __builtin_amdgcn_mov_dpp(__builtin_bit_cast(int, x), CTRL, 0xf, 0xf, true));
}
DI float row_sum16(float x) {
  x += dpp<0x128>(x); x += dpp<0x124>(x); x += dpp<0x122>(x); x += dpp<0x121>(x); return x;
}
DI float ex2(float x) { return __builtin_amdgcn_exp2f(x); }
template <typename T> DI T ntload(const void* p) { return __builtin_nontemporal_load(reinterpret_cast<const T*>(p)); }
#define MFMA32(a, b, c) __builtin_amdgcn_mfma_f32_32x32x16_bf16(__builtin_bit_cast(bf16x8, a), __builtin_bit_cast(bf16x8, b), (c), 0, 0, 0)
DI u32x4 pack8(const f32x16& x, int s) {
  u32x4 p;
#pragma unroll
  for (int j = 0; j < 4; ++j) p[j] = pack2(x[8 * s + 2 * j], x[8 * s + 2 * j + 1]);
  return p;
}
DI int opaque(int x) { asm volatile("" : "+v"(x)); return x; }
#define VM0() asm volatile("s_waitcnt vmcnt(0)" ::: "memory")
#define CBAR() asm volatile("" ::: "memory")
#define WGBAR() do { asm volatile("s_waitcnt lgkmcnt(0)" ::: "memory"); __builtin_amdgcn_s_barrier(); asm volatile("" ::: "memory"); } while (0)

#define STAMP(k) do { } while (0)

DI void st_granule(unsigned char* ws, size_t off, float v) {
  __hip_atomic_store((GAS u64*)(ws + off), (1ull << 32) | (u64)asu(v), RLX_AGENT);
}
DI u64 ld_granule(unsigned char* ws, size_t off) { return __hip_atomic_load((GAS u64*)(ws + off), RLX_AGENT); }
DI bool spin_fail(unsigned& spins, unsigned char* ws) {
  __builtin_amdgcn_s_sleep(1);
  ++spins;
  if (spins > kSpinCap) { __hip_atomic_store((GAS unsigned*)(ws + Z_TMO), 1u, RLX_AGENT); return true; }
  if ((spins & 255u) == 0u) return __builtin_amdgcn_readfirstlane(__hip_atomic_load((GAS unsigned*)(ws + Z_TMO), RLX_AGENT)) != 0u;
  return false;
}
template <int N> DI void gather(unsigned char* ws, size_t off, size_t stride, bool active, float (&v)[N]) {
  for (unsigned spins = 0;;) {
    bool ok = true;
    if (active) {
#pragma unroll
      for (int k = 0; k < N; ++k) {
        const u64 x = ld_granule(ws, off + k * stride);
        v[k] = asf((unsigned)x);
        ok &= (x >> 32) == 1ull;
      }
    }
    if (__all(ok)) break;
    if (spin_fail(spins, ws)) break;
  }
  CBAR();
}
DI void wait_flags64(unsigned char* ws, size_t off, int lane) {
  for (unsigned spins = 0;;) {
    const unsigned f = __hip_atomic_load((GAS unsigned*)(ws + off) + lane, RLX_AGENT);
    if (__all(f != 0u)) break;
    if (spin_fail(spins, ws)) break;
  }
  CBAR();
}

DI void fma_row(float (&acc)[12], float p, const u32x4& a, const u32x2& b) {
#pragma unroll
  for (int k = 0; k < 4; ++k) { acc[2 * k] = fmaf(p, bflo(a[k]), acc[2 * k]); acc[2 * k + 1] = fmaf(p, bfhi(a[k]), acc[2 * k + 1]); }
#pragma unroll
  for (int k = 0; k < 2; ++k) { acc[8 + 2 * k] = fmaf(p, bflo(b[k]), acc[8 + 2 * k]); acc[8 + 2 * k + 1] = fmaf(p, bfhi(b[k]), acc[8 + 2 * k + 1]); }
}
DI void rows_to_lds(float (&acc)[12], LAS float* dst, int lane) {
  const int r = lane >> 4, c = lane & 15;
#pragma unroll
  for (int k = 0; k < 12; ++k) { float a = acc[k]; a += __shfl_xor(a, 16); a += __shfl_xor(a, 32); acc[k] = a; }
  if (r == 0) {
#pragma unroll
    for (int k = 0; k < 8; ++k) dst[8 * c + k] = acc[k];
#pragma unroll
    for (int k = 0; k < 4; ++k) dst[128 + 4 * c + k] = acc[8 + k];
  }
}

DI void out_reduce(unsigned char* ws, u16* __restrict__ out, LAS unsigned char* L, int mat, int col0, int tid) {
  const int col = tid & 127, qd = tid >> 7;
  float v[8];
  gather<8>(ws, Z_GOUT + (((size_t)mat * NH + qd * 8) * DM + col0 + col) * 8, (size_t)DM * 8, true, v);
  LAS float* red = (LAS float*)(L + L_RED);
  red[tid] = ((v[0] + v[1]) + (v[2] + v[3])) + ((v[4] + v[5]) + (v[6] + v[7]));
  WGBAR();
  if (tid < 128) out[mat * DM + col0 + tid] = f2bf((red[tid] + red[128 + tid]) + (red[256 + tid] + red[384 + tid]));
}

struct QLoads { u32x4 a[5]; u32x2 b[5]; float x[5]; };
DI void q_issue(QLoads& Q, const u16* __restrict__ xA, const u16* __restrict__ wqA, const u16* __restrict__ xB,
                const u16* __restrict__ wqB, int h, int jj, int wave, int lane) {
  const int r = lane >> 4, c = lane & 15;
  const u16* x = wave < 4 ? xA : xB;
  const u16* w = wave < 4 ? wqA : wqB;
#pragma unroll
  for (int i = 0; i < 5; ++i) {
    const int row = 80 * jj + 20 * (wave & 3) + 4 * i + r;
    const u16* p = w + (size_t)row * HD + h * DH;
    Q.a[i] = ntload<u32x4>(p + 8 * c); Q.b[i] = ntload<u32x2>(p + 128 + 4 * c);
    Q.x[i] = bflo((unsigned)x[row]);
  }
}
DI void q_partial(QLoads& Q, LAS unsigned char* L, int wave, int lane) {
  float acc[12];
#pragma unroll
  for (int k = 0; k < 12; ++k) acc[k] = 0.f;
#pragma unroll
  for (int i = 0; i < 5; ++i) fma_row(acc, Q.x[i], Q.a[i], Q.b[i]);
  rows_to_lds(acc, (LAS float*)(L + L_RED) + wave * DH, lane);
}
DI void q_exchange(unsigned char* ws, LAS unsigned char* L, int h, int jj, int part, int wave, int lane) {
  const int tid = wave * 64 + lane;
  const LAS float* red = (const LAS float*)(L + L_RED);
  if (tid < 384) {
    const int mt = tid >= DH ? 1 : 0, d = tid - mt * DH;
    const LAS float* rp = red + mt * 4 * DH + d;
    st_granule(ws, Z_GQP + (((size_t)h * 8 + jj) * 384 + tid) * 8, (rp[0] + rp[DH]) + (rp[2 * DH] + rp[3 * DH]));
  }
  if (wave < 3) {
    float v[8];
    gather<8>(ws, Z_GQP + ((size_t)h * 8 * 384 + part * DH + tid) * 8, 384 * 8, true, v);
    ((LAS u16*)(L + L_QS))[tid] = f2bf(((v[0] + v[1]) + (v[2] + v[3])) + ((v[4] + v[5]) + (v[6] + v[7])));
  }
}

DI void a_role(const u16* __restrict__ xA, const u16* __restrict__ wqA, const u16* __restrict__ xB, const u16* __restrict__ wqB,
               const u16* __restrict__ K, const u16* __restrict__ V, const u16* __restrict__ woA,
               unsigned char* ws, u16* __restrict__ out, LAS unsigned char* L, int h, int j, int wave, int lane) {
  const int r = lane >> 4, c = lane & 15, tid = wave * 64 + lane;
  STAMP(0);
  QLoads Q;
  q_issue(Q, xA, wqA, xB, wqB, h, j, wave, lane);
  constexpr int KR = A_KR, NB = A_NB, TK = 4 * KR, NTH = SK / TK;
  const int gw = j * 8 + wave;
  const int tb = (NTH * gw) / 48, te = (NTH * (gw + 1)) / 48, NT = te - tb;
  const size_t base = ((size_t)h * SK + tb * TK + r) * DH;
  const u16* kp = K + base;
  const u16* vp = V + base;
  u32x4 ka[NB][KR], va[NB][KR]; u32x2 kb[NB][KR], vb[NB][KR];
#define LOADT(buf, t) do { _Pragma("unroll") for (int i = 0; i < KR; ++i) { \
    const u16* kr = kp + (size_t)((t) * TK + i * 4) * DH; const u16* vr = vp + (size_t)((t) * TK + i * 4) * DH; \
    ka[buf][i] = ntload<u32x4>(kr + 8 * c); kb[buf][i] = ntload<u32x2>(kr + 128 + 4 * c); \
    va[buf][i] = ntload<u32x4>(vr + 8 * c); vb[buf][i] = ntload<u32x2>(vr + 128 + 4 * c); } } while (0)
  q_partial(Q, L, wave, lane);
  STAMP(10);
  WGBAR();
  STAMP(11);
  q_exchange(ws, L, h, j, 0, wave, lane);
  STAMP(12);
  __builtin_amdgcn_sched_barrier(0);
  WGBAR();
#pragma unroll
  for (int b = 0; b < NB; ++b) LOADT(b, b);
  __builtin_amdgcn_sched_barrier(0);
  STAMP(1);
  const u32x4 qa = *(const LAS u32x4*)(L + L_QS + 16 * c);
  const u32x2 qb = *(const LAS u32x2*)(L + L_QS + 256 + 8 * c);
  float m = -INFINITY, l = 0.f, acc[12];
#pragma unroll
  for (int k = 0; k < 12; ++k) acc[k] = 0.f;
  for (int t0 = 0; t0 < NT; t0 += NB) {
#pragma unroll
    for (int b = 0; b < NB; ++b) {
      const int t = t0 + b;
      if (t < NT) {
        float s[KR];
        float mn = m;
#pragma unroll
        for (int i = 0; i < KR; ++i) {
          float d = 0.f;
#pragma unroll
          for (int k = 0; k < 4; ++k) d = dot2(ka[b][i][k], qa[k], d);
          d = dot2(kb[b][i][0], qb[0], d); d = dot2(kb[b][i][1], qb[1], d);
          d = row_sum16(d);
          s[i] = rbf(rbf(d) * SCALE);
          mn = fmaxf(mn, s[i]);
        }
        const float alpha = ex2((m - mn) * LOG2E);
        m = mn;
        float ps = 0.f;
#pragma unroll
        for (int i = 0; i < KR; ++i) { s[i] = ex2((s[i] - mn) * LOG2E); ps += s[i]; }
        l = l * alpha + ps;
#pragma unroll
        for (int k = 0; k < 12; ++k) acc[k] *= alpha;
#pragma unroll
        for (int i = 0; i < KR; ++i) fma_row(acc, s[i], va[b][i], vb[b][i]);
        if (t + NB < NT) LOADT(b, t + NB);
      }
    }
  }
#undef LOADT
  STAMP(2);
  u32x4 wv[6];
  if (j < 5) {
#pragma unroll
    for (int i = 0; i < 6; ++i) wv[i] = ntload<u32x4>(woA + (size_t)(h * DH + 24 * wave + 4 * i + r) * DM + 128 * j + 8 * c);
  }
  float M = fmaxf(m, __shfl_xor(m, 16)); M = fmaxf(M, __shfl_xor(M, 32));
  const float f = ex2((m - M) * LOG2E);
  l *= f; l += __shfl_xor(l, 16); l += __shfl_xor(l, 32);
#pragma unroll
  for (int k = 0; k < 12; ++k) acc[k] *= f;
  LAS float* red = (LAS float*)(L + L_RED);
  rows_to_lds(acc, red + wave * 200 + 8, lane);
  if (lane == 0) { red[wave * 200] = M; red[wave * 200 + 1] = l; }
  WGBAR();
  {
    float mw[8], MM = -INFINITY;
#pragma unroll
    for (int w = 0; w < 8; ++w) { mw[w] = red[w * 200]; MM = fmaxf(MM, mw[w]); }
    const int idx = tid < DH ? 8 + tid : 1;
    float s = 0.f;
#pragma unroll
    for (int w = 0; w < 8; ++w) s = fmaf(ex2((mw[w] - MM) * LOG2E), red[w * 200 + idx], s);
    const size_t ga = Z_GAP + ((size_t)h * NA + j) * 200 * 8;
    if (tid < DH) st_granule(ws, ga + (size_t)(8 + tid) * 8, s);
    if (tid == DH) st_granule(ws, ga + 8, s);
    if (tid == DH + 1) st_granule(ws, ga, MM);
  }
  if (j < 5) {
    if (wave < 3) {
      float v[NA], mm[NA], ll[NA];
      const size_t gh = Z_GAP + (size_t)h * NA * 200 * 8;
      for (unsigned spins = 0;;) {
        bool ok = true;
#pragma unroll
        for (int k = 0; k < NA; ++k) {
          const u64 x = ld_granule(ws, gh + (size_t)(k * 200 + 8 + tid) * 8), y = ld_granule(ws, gh + (size_t)(k * 200) * 8),
                    z = ld_granule(ws, gh + (size_t)(k * 200 + 1) * 8);
          v[k] = asf((unsigned)x); mm[k] = asf((unsigned)y); ll[k] = asf((unsigned)z);
          ok &= ((x & y & z) >> 32) == 1ull;
        }
        if (__all(ok)) break;
        if (spin_fail(spins, ws)) break;
      }
      CBAR();
      float Mx = mm[0];
#pragma unroll
      for (int k = 1; k < NA; ++k) Mx = fmaxf(Mx, mm[k]);
      float Lsum = 0.f, o = 0.f;
#pragma unroll
      for (int k = 0; k < NA; ++k) { const float g = ex2((mm[k] - Mx) * LOG2E); Lsum = fmaf(g, ll[k], Lsum); o = fmaf(g, v[k], o); }
      ((LAS float*)(L + L_VEC))[tid] = rbf(o / Lsum);
    }
    WGBAR();
    STAMP(3);
    const LAS float* vec = (const LAS float*)(L + L_VEC);
    float a8[8];
#pragma unroll
    for (int k = 0; k < 8; ++k) a8[k] = 0.f;
#pragma unroll
    for (int i = 0; i < 6; ++i) {
      const float x = vec[24 * wave + 4 * i + r];
#pragma unroll
      for (int k = 0; k < 4; ++k) { a8[2 * k] = fmaf(x, bflo(wv[i][k]), a8[2 * k]); a8[2 * k + 1] = fmaf(x, bfhi(wv[i][k]), a8[2 * k + 1]); }
    }
#pragma unroll
    for (int k = 0; k < 8; ++k) { a8[k] += __shfl_xor(a8[k], 16); a8[k] += __shfl_xor(a8[k], 32); }
    LAS float* r2 = (LAS float*)(L + L_BIG);
    if (r == 0) {
#pragma unroll
      for (int k = 0; k < 8; ++k) r2[wave * 128 + 8 * c + k] = a8[k];
    }
    WGBAR();
    if (tid < 128) {
      float s = 0.f;
#pragma unroll
      for (int w = 0; w < 8; ++w) s += r2[w * 128 + tid];
      st_granule(ws, Z_GOUT + ((size_t)h * DM + 128 * j + tid) * 8, s);
    }
    STAMP(4);
    if (h == 4 * j + 1) out_reduce(ws, out, L, 0, 128 * j, tid);
    STAMP(5);
  }
}

DI void b_role(const u16* __restrict__ xA, const u16* __restrict__ wqA, const u16* __restrict__ xB, const u16* __restrict__ wqB,
               const u16* __restrict__ ckv, const u16* __restrict__ wuk, const u16* __restrict__ wuv, const u16* __restrict__ woB,
               unsigned char* ws, u16* __restrict__ out, LAS unsigned char* L, int h, int half, int wave, int lane) {
  const int r = lane >> 4, c = lane & 15, tid = wave * 64 + lane;
  const int cl = lane & 31, hh = lane >> 5;
  const int kb_ = 2 * h + half;
  const __amdgpu_buffer_rsrc_t rs = __builtin_amdgcn_make_buffer_rsrc((void*)ws, 0, (int)W_END, 0x00020000);
  STAMP(0);
  u32x4 cf[4][6];
  {
    QLoads Q;
    q_issue(Q, xA, wqA, xB, wqB, h, NA + half, wave, lane);
    u32x4 ua[12]; u32x2 ub[12];
#define LOADU() do { _Pragma("unroll") for (int i = 0; i < 12; ++i) { \
      const u16* row = wuk + ((size_t)(384 * half + 48 * wave + 4 * i + r) * NH + h) * DH; \
      ua[i] = ntload<u32x4>(row + 8 * c); ub[i] = ntload<u32x2>(row + 128 + 4 * c); } } while (0)
#define LOADC() do { _Pragma("unroll") for (int t = 0; t < 4; ++t) _Pragma("unroll") for (int i = 0; i < 6; ++i) \
        cf[t][i] = ntload<u32x4>(ckv + (size_t)(128 * kb_ + 32 * t) * DC + (size_t)(384 * wave + 64 * i + lane) * 8); } while (0)
    __builtin_amdgcn_sched_barrier(0);
    q_partial(Q, L, wave, lane);
    STAMP(13);
    WGBAR();
    STAMP(14);
    __builtin_amdgcn_sched_barrier(0);
    if (wave >= 3) LOADU();
    __builtin_amdgcn_sched_barrier(0);
    q_exchange(ws, L, h, NA + half, 1, wave, lane);
    STAMP(15);
    __builtin_amdgcn_sched_barrier(0);
    if (wave < 3) LOADU();
    if (wave >= 2) LOADC();
    __builtin_amdgcn_sched_barrier(0);
    WGBAR();
    STAMP(1);
    const u32x4 qa = *(const LAS u32x4*)(L + L_QS + 16 * c);
    const u32x2 qb = *(const LAS u32x2*)(L + L_QS + 256 + 8 * c);
    LAS float* qv = (LAS float*)(L + L_VEC);
#pragma unroll
    for (int i = 0; i < 12; ++i) {
      float s = 0.f;
#pragma unroll
      for (int k = 0; k < 4; ++k) s = dot2(ua[i][k], qa[k], s);
      s = dot2(ub[i][0], qb[0], s); s = dot2(ub[i][1], qb[1], s);
      s = row_sum16(s);
      if (c == 0) qv[48 * wave + 4 * i + r] = s;
    }
    WGBAR();
    if (wave < 2) {
      if (lane < 48) {
        u32x4 o;
#pragma unroll
        for (int k = 0; k < 4; ++k) {
          const float a = qv[8 * lane + 2 * k], b = qv[8 * lane + 2 * k + 1];
          const float ah = rbf(a), bh = rbf(b);
          o[k] = wave ? pack2(a - ah, b - bh) : pack2(ah, bh);
        }
        const int c8 = 48 * half + lane;
        __builtin_amdgcn_raw_buffer_store_b128(o, rs, (int)(wave ? W_QL : W_QH) + ((c8 >> 1) * 64 + h + 32 * (c8 & 1)) * 16, 0, 16);
      }
      VM0();
      if (lane == 0) __hip_atomic_store((GAS unsigned*)(ws + Z_FQABS) + 2 * kb_ + wave, 1u, RLX_AGENT);
      __builtin_amdgcn_sched_barrier(0);
      LOADC();
    }
#undef LOADC
#undef LOADU
  }
  STAMP(2);
#pragma unroll
  for (int rd = 0; rd < 2; ++rd) {
#pragma unroll
    for (int tt = 0; tt < 2; ++tt)
#pragma unroll
      for (int i = 0; i < 6; ++i) {
        const int e = 384 * wave + 64 * i + lane;
        *(LAS u32x4*)(L + L_BIG + tt * 49664 + (e / 96) * 1552 + (e % 96) * 16) = cf[2 * rd + tt][i];
      }
    WGBAR();
#pragma unroll
    for (int tt = 0; tt < 2; ++tt)
#pragma unroll
      for (int s = 0; s < 6; ++s)
        cf[2 * rd + tt][s] = *(const LAS u32x4*)(L + L_BIG + tt * 49664 + cl * 1552 + (96 * wave + 16 * s + 8 * hh) * 2);
    WGBAR();
  }
  {
    for (unsigned spins = 0;;) {
      const u32x2 f = __builtin_amdgcn_raw_buffer_load_b64(rs, (int)Z_FQABS + lane * 8, 0, 16);
      if (__all((f[0] != 0u) & (f[1] != 0u))) break;
      if (spin_fail(spins, ws)) break;
    }
    CBAR();
    STAMP(3);
    u32x4 qh[6], ql[6];
#pragma unroll
    for (int s = 0; s < 6; ++s) {
      qh[s] = __builtin_amdgcn_raw_buffer_load_b128(rs, (int)W_QH + ((wave * 6 + s) * 64 + lane) * 16, 0, 16);
      ql[s] = __builtin_amdgcn_raw_buffer_load_b128(rs, (int)W_QL + ((wave * 6 + s) * 64 + lane) * 16, 0, 16);
    }
    __builtin_amdgcn_sched_barrier(0);
    f32x16 zero;
#pragma unroll
    for (int i = 0; i < 16; ++i) zero[i] = 0.f;
    LAS f32x4* sp = (LAS f32x4*)(L + L_BIG);
#pragma unroll
    for (int t = 0; t < 4; ++t) {
      f32x16 S = zero;
#pragma unroll
      for (int s = 0; s < 6; ++s) S = MFMA32(cf[t][s], ql[s], S);
#pragma unroll
      for (int s = 0; s < 6; ++s) S = MFMA32(cf[t][s], qh[s], S);
#pragma unroll
      for (int g = 0; g < 4; ++g) sp[((wave * 4 + t) * 4 + g) * 64 + lane] = f32x4{S[4 * g], S[4 * g + 1], S[4 * g + 2], S[4 * g + 3]};
    }
    STAMP(4);
    WGBAR();
    float sv[8];
    {
      const int t = wave >> 1;
#pragma unroll
      for (int gg = 0; gg < 2; ++gg) {
        const int g = 2 * (wave & 1) + gg;
        f32x4 a = sp[((0 * 4 + t) * 4 + g) * 64 + lane];
#pragma unroll
        for (int w = 1; w < 8; ++w) a += sp[((w * 4 + t) * 4 + g) * 64 + lane];
#pragma unroll
        for (int k = 0; k < 4; ++k) sv[4 * gg + k] = rbf(rbf(a[k]) * SCALE);
      }
    }
    float m = fmaxf(fmaxf(fmaxf(sv[0], sv[1]), fmaxf(sv[2], sv[3])), fmaxf(fmaxf(sv[4], sv[5]), fmaxf(sv[6], sv[7])));
    m = fmaxf(m, __shfl_xor(m, 32));
    LAS float* mx = (LAS float*)(L + L_RED);
    if (lane < 32) mx[wave * 32 + lane] = m;
    WGBAR();
#pragma unroll
    for (int w = 0; w < 8; ++w) m = fmaxf(m, mx[w * 32 + cl]);
    float l = 0.f;
#pragma unroll
    for (int k = 0; k < 8; ++k) { sv[k] = ex2((sv[k] - m) * LOG2E); l += sv[k]; }
    l += __shfl_xor(l, 32);
    if (lane < 32) mx[256 + wave * 32 + lane] = l;
    LAS u32x4* pf = (LAS u32x4*)(L + L_SF);
    pf[wave * 64 + lane] = u32x4{pack2(sv[0], sv[1]), pack2(sv[2], sv[3]), pack2(sv[4], sv[5]), pack2(sv[6], sv[7])};
    WGBAR();
    u32x4 pb[4][2];
#pragma unroll
    for (int t = 0; t < 4; ++t) { pb[t][0] = pf[(2 * t) * 64 + lane]; pb[t][1] = pf[(2 * t + 1) * 64 + lane]; }
    if (wave == 0 && lane < 32) {
      float ls = 0.f;
#pragma unroll
      for (int w = 0; w < 8; ++w) ls += mx[256 + w * 32 + lane];
      const u32x2 ml = {asu(m), asu(ls)};
      __builtin_amdgcn_raw_buffer_store_b64(ml, rs, (int)W_BML + ((cl * NSB + kb_) * 2) * 4, 0, 16);
    }
    u32x4 idlo, idhi;
#pragma unroll
    for (int k = 0; k < 4; ++k) {
      const int k0 = 8 * hh + 2 * k, k1 = k0 + 1;
      idlo[k] = (cl == k0 ? 0x3f80u : 0u) | (cl == k1 ? 0x3f800000u : 0u);
      idhi[k] = (cl == k0 + 16 ? 0x3f80u : 0u) | (cl == k1 + 16 ? 0x3f800000u : 0u);
    }
    STAMP(5);
    LAS float* ost = (LAS float*)(L + L_BIG) + wave * 3200;
#pragma unroll
    for (int ct = 0; ct < 3; ++ct) {
      f32x16 O = zero;
#pragma unroll
      for (int t = 0; t < 4; ++t) {
        f32x16 T = MFMA32(cf[t][2 * ct], idlo, zero);
        T = MFMA32(cf[t][2 * ct + 1], idhi, T);
        O = MFMA32(pack8(T, 0), pb[t][0], O);
        O = MFMA32(pack8(T, 1), pb[t][1], O);
      }
#pragma unroll
      for (int g = 0; g < 4; ++g)
        *(LAS f32x4*)(ost + cl * 100 + 32 * ct + 8 * g + 4 * hh) = f32x4{O[4 * g], O[4 * g + 1], O[4 * g + 2], O[4 * g + 3]};
    }
    asm volatile("s_waitcnt lgkmcnt(0)" ::: "memory");
#pragma unroll
    for (int i = 0; i < 12; ++i) {
      const int e = i * 64 + lane, hd = e / 24, q4 = e % 24;
      const f32x4 v = *(const LAS f32x4*)(ost + hd * 100 + 4 * q4);
      __builtin_amdgcn_raw_buffer_store_b128(__builtin_bit_cast(u32x4, v), rs, (int)W_BO + ((kb_ * NH + hd) * DC + 96 * wave + 4 * q4) * 4, 0, 16);
    }
    STAMP(6);
    VM0();
    WGBAR();
    if (tid == 0) __hip_atomic_store((GAS unsigned*)(ws + Z_FBATT) + kb_, 1u, RLX_AGENT);
    STAMP(7);
  }
  {
    const int lane2 = opaque(lane);
    const int r = lane2 >> 4, c = lane2 & 15, tid = wave * 64 + lane2;
    if (wave != 0) {
      __builtin_amdgcn_sched_barrier(0);
    }
    u32x4 va[12]; u32x2 vb[12];
#pragma unroll
    for (int i = 0; i < 12; ++i) {
      const u16* row = wuv + ((size_t)(384 * half + 48 * wave + 4 * i + r) * NH + h) * DH;
      va[i] = ntload<u32x4>(row + 8 * c); vb[i] = ntload<u32x2>(row + 128 + 4 * c);
    }
    __builtin_amdgcn_sched_barrier(0);
    wait_flags64(ws, Z_FBATT, lane2);
    STAMP(8);
    const u32x2 st = __builtin_amdgcn_raw_buffer_load_b64(rs, (int)W_BML + (h * NSB + lane2) * 8, 0, 16);
    f32x4 pa[8], pc[8];
    const int l32 = lane2 & 31;
#pragma unroll
    for (int i = 0; i < 8; ++i) {
      const int o = (int)W_BO + (((8 * wave + i) * NH + h) * DC + 384 * half) * 4;
      pa[i] = __builtin_bit_cast(f32x4, __builtin_amdgcn_raw_buffer_load_b128(rs, o + lane2 * 16, 0, 16));
      pc[i] = __builtin_bit_cast(f32x4, __builtin_amdgcn_raw_buffer_load_b128(rs, o + (64 + l32) * 16, 0, 16));
    }
    const float m0 = asf(st[0]), l0 = asf(st[1]);
    float M = m0;
#pragma unroll
    for (int o = 1; o < 64; o <<= 1) M = fmaxf(M, __shfl_xor(M, o));
    const float f0 = ex2((m0 - M) * LOG2E);
    float Ls = f0 * l0;
#pragma unroll
    for (int o = 1; o < 64; o <<= 1) Ls += __shfl_xor(Ls, o);
    const float w0 = f0 / Ls;
    f32x4 sa = {0.f, 0.f, 0.f, 0.f}, sc = {0.f, 0.f, 0.f, 0.f};
#pragma unroll
    for (int i = 0; i < 8; ++i) { const float w = __shfl(w0, 8 * wave + i); sa += w * pa[i]; sc += w * pc[i]; }
    LAS f32x4* acc4 = (LAS f32x4*)(L + L_RED);
    acc4[wave * 96 + lane2] = sa;
    if (lane2 < 32) acc4[wave * 96 + 64 + lane2] = sc;
    __builtin_amdgcn_sched_barrier(0);
    u32x4 wo[24];
    const int lc = lane2 < 40 ? lane2 : 0;
#pragma unroll
    for (int i = 0; i < 24; ++i) wo[i] = ntload<u32x4>(woB + (size_t)(h * DH + 24 * wave + i) * DM + 320 * half + 8 * lc);
    __builtin_amdgcn_sched_barrier(0);
    WGBAR();
    if (tid < 384) {
      const LAS float* af = (const LAS float*)(L + L_RED) + tid;
      float s = 0.f;
#pragma unroll
      for (int w = 0; w < 8; ++w) s += af[w * 384];
      ((LAS float*)(L + L_VEC))[tid] = s;
    }
    WGBAR();
    STAMP(9);
    float acc[12];
#pragma unroll
    for (int k = 0; k < 12; ++k) acc[k] = 0.f;
    const LAS float* cv = (const LAS float*)(L + L_VEC);
#pragma unroll
    for (int i = 0; i < 12; ++i) fma_row(acc, cv[48 * wave + 4 * i + r], va[i], vb[i]);
    LAS float* red = (LAS float*)(L + L_RED);
    rows_to_lds(acc, red + wave * DH, lane2);
    WGBAR();
    if (tid < DH) {
      float s = 0.f;
#pragma unroll
      for (int w = 0; w < 8; ++w) s += red[w * DH + tid];
      st_granule(ws, Z_GCTX + (((size_t)h * 2 + half) * DH + tid) * 8, s);
      float v[1];
      gather<1>(ws, Z_GCTX + (((size_t)h * 2 + (half ^ 1)) * DH + tid) * 8, 0, true, v);
      ((LAS float*)(L + L_VEC))[tid] = rbf(half ? v[0] + s : s + v[0]);
    }
    WGBAR();
    STAMP(10);
    float a8[8];
#pragma unroll
    for (int k = 0; k < 8; ++k) a8[k] = 0.f;
#pragma unroll
    for (int i = 0; i < 24; ++i) {
      const float x = cv[24 * wave + i];
#pragma unroll
      for (int k = 0; k < 4; ++k) { a8[2 * k] = fmaf(x, bflo(wo[i][k]), a8[2 * k]); a8[2 * k + 1] = fmaf(x, bfhi(wo[i][k]), a8[2 * k + 1]); }
    }
    LAS float* r2 = (LAS float*)(L + L_BIG);
    if (lane2 < 40) {
#pragma unroll
      for (int k = 0; k < 8; ++k) r2[wave * 320 + 8 * lane2 + k] = a8[k];
    }
    WGBAR();
    if (tid < 320) {
      float s = 0.f;
#pragma unroll
      for (int w = 0; w < 8; ++w) s += r2[w * 320 + tid];
      st_granule(ws, Z_GOUT + (((size_t)NH + h) * DM + 320 * half + tid) * 8, s);
    }
    STAMP(11);
    if ((kb_ % 12) == 3 && kb_ < 60) out_reduce(ws, out, L, 1, 128 * (kb_ / 12), tid);
    STAMP(12);
  }
}

__global__ void __launch_bounds__(512, 2) mega(const u16* __restrict__ xA, const u16* __restrict__ K, const u16* __restrict__ V,
                                               const u16* __restrict__ wqA, const u16* __restrict__ woA,
                                               const u16* __restrict__ xB, const u16* __restrict__ ckv, const u16* __restrict__ wqB,
                                               const u16* __restrict__ wuk, const u16* __restrict__ wuv, const u16* __restrict__ woB,
                                               unsigned char* ws, u16* __restrict__ out) {
  extern __shared__ __attribute__((aligned(16))) unsigned char smem[];
  LAS unsigned char* L = (LAS unsigned char*)smem;
  const int tid = threadIdx.x, lane = tid & 63;
  const int wave = __builtin_amdgcn_readfirstlane(tid >> 6);
  const int b = blockIdx.x;
  const int h = (b & 7) * 4 + (b >> 6), jj = (b >> 3) & 7;
  if (jj < NA) a_role(xA, wqA, xB, wqB, K, V, woA, ws, out, L, h, jj, wave, lane);
  else b_role(xA, wqA, xB, wqB, ckv, wuk, wuv, woB, ws, out, L, h, jj - NA, wave, lane);
}

extern "C" void kernel_launch(void* const* d_in, const int* in_sizes, int n_in,
                              void* d_out, int out_size, void* d_ws, size_t ws_size,
                              hipStream_t stream) {
  (void)hipFuncSetAttribute((const void*)mega, hipFuncAttributeMaxDynamicSharedMemorySize, L_BYTES);
  (void)hipMemsetAsync(d_ws, 0, Z_END, stream);
  mega<<<256, 512, L_BYTES, stream>>>((const u16*)d_in[0], (const u16*)d_in[1], (const u16*)d_in[2], (const u16*)d_in[3], (const u16*)d_in[4],
                                      (const u16*)d_in[5], (const u16*)d_in[6], (const u16*)d_in[7], (const u16*)d_in[8], (const u16*)d_in[9],
                                      (const u16*)d_in[10], (unsigned char*)d_ws, (u16*)d_out);
}
```

```cpp
#define USE_MEMSET 1
#include <hip/hip_runtime.h>
#include <hip/hip_bf16.h>
#include <stdint.h>

typedef unsigned u32x4 __attribute__((ext_vector_type(4)));
typedef unsigned u32x2 __attribute__((ext_vector_type(2)));
typedef __bf16 bf16x2 __attribute__((ext_vector_type(2)));
typedef __bf16 bf16x8 __attribute__((ext_vector_type(8)));
typedef float f32x16 __attribute__((ext_vector_type(16)));
typedef float f32x4 __attribute__((ext_vector_type(4)));
typedef unsigned short u16;
typedef unsigned long long u64;

#define DI __device__ __forceinline__
#define GAS __attribute__((address_space(1)))
#define LAS __attribute__((address_space(3)))
#define RLX_AGENT __ATOMIC_RELAXED, __HIP_MEMORY_SCOPE_AGENT

constexpr int NH = 32, SK = 8192, DH = 192, DM = 640, DC = 768, HD = NH * DH;
constexpr float SCALE = 0.07216878364870322f;
constexpr float LOG2E = 1.4426950408889634f;
#define A_KR 2
#define A_NB 1
#define EARLY_W 3
constexpr int NA = 6;
constexpr int NSB = 64;
constexpr unsigned kSpinCap = 1u << 18;

constexpr size_t Z_TMO = 0;
constexpr size_t Z_FQABS = 1024;
constexpr size_t Z_FBATT = 2048;
constexpr size_t Z_TCNT = 4096;
constexpr size_t Z_GQP = 8192;
constexpr size_t Z_GCTX = Z_GQP + (size_t)NH * 5 * 384 * 8;
constexpr size_t Z_GAP = Z_GCTX + (size_t)NH * 2 * 192 * 8;
constexpr size_t Z_GOUT = Z_GAP + (size_t)NH * NA * 200 * 8;
constexpr size_t Z_END = Z_GOUT + (size_t)2 * NH * DM * 8;
constexpr size_t W_QH = Z_END;
constexpr size_t W_QL = W_QH + (size_t)NH * DC * 2;
constexpr size_t W_BML = W_QL + (size_t)NH * DC * 2;
constexpr size_t W_BO = W_BML + (size_t)NH * NSB * 2 * 4;
constexpr size_t W_END = W_BO + (size_t)NSB * NH * DC * 4;
static_assert(Z_END % 16 == 0 && W_END < (1ull << 31), "ws layout");

constexpr int L_QS = 0;
constexpr int L_VEC = 512;
constexpr int L_RED = 2048;
constexpr int L_BIG = 16384;
constexpr int L_SF = L_BIG + 131072;
constexpr int L_BYTES = L_SF + 16384;

DI float dot2(unsigned a, unsigned b, float c) {
  return __builtin_amdgcn_fdot2_f32_bf16(__builtin_bit_cast(bf16x2, a), __builtin_bit_cast(bf16x2, b), c, false);
}
DI float rbf(float x) { return (float)(__bf16)x; }
DI float asf(unsigned v) { return __builtin_bit_cast(float, v); }
DI unsigned asu(float v) { return __builtin_bit_cast(unsigned, v); }
DI float bflo(unsigned v) { return __builtin_bit_cast(float, v << 16); }
DI float bfhi(unsigned v) { return __builtin_bit_cast(float, v & 0xffff0000u); }
DI u16 f2bf(float x) { return __builtin_bit_cast(u16, (__bf16)x); }
DI unsigned pack2(float lo, float hi) { return (unsigned)f2bf(lo) | ((unsigned)f2bf(hi) << 16); }
template <int CTRL> DI float dpp(float x) {
  return __builtin_bit_cast(float, __builtin_amdgcn_mov_dpp(__builtin_bit_cast(int, x), CTRL, 0xf, 0xf, true));
}
DI float row_sum16(float x) {
  x += dpp<0x128>(x); x += dpp<0x124>(x); x += dpp<0x122>(x); x += dpp<0x121>(x); return x;
}
DI float ex2(float x) { return __builtin_amdgcn_exp2f(x); }
template <typename T> DI T ntload(const void* p) { return __builtin_nontemporal_load(reinterpret_cast<const T*>(p)); }
#define MFMA32(a, b, c) __builtin_amdgcn_mfma_f32_32x32x16_bf16(__builtin_bit_cast(bf16x8, a), __builtin_bit_cast(bf16x8, b), (c), 0, 0, 0)
DI u32x4 pack8(const f32x16& x, int s) {
  u32x4 p;
#pragma unroll
  for (int j = 0; j < 4; ++j) p[j] = pack2(x[8 * s + 2 * j], x[8 * s + 2 * j + 1]);
  return p;
}
DI int opaque(int x) { asm volatile("" : "+v"(x)); return x; }
#define VM0() asm volatile("s_waitcnt vmcnt(0)" ::: "memory")
#define CBAR() asm volatile("" ::: "memory")
#define WGBAR() do { asm volatile("s_waitcnt lgkmcnt(0)" ::: "memory"); __builtin_amdgcn_s_barrier(); asm volatile("" ::: "memory"); } while (0)

#define STAMP(k) do { } while (0)

DI void st_granule(unsigned char* ws, size_t off, float v) {
  __hip_atomic_store((GAS u64*)(ws + off), (1ull << 32) | (u64)asu(v), RLX_AGENT);
}
DI u64 ld_granule(unsigned char* ws, size_t off) { return __hip_atomic_load((GAS u64*)(ws + off), RLX_AGENT); }
DI bool spin_fail(unsigned& spins, unsigned char* ws) {
  __builtin_amdgcn_s_sleep(1);
  ++spins;
  if (spins > kSpinCap) { __hip_atomic_store((GAS unsigned*)(ws + Z_TMO), 1u, RLX_AGENT); return true; }
  if ((spins & 255u) == 0u) return __builtin_amdgcn_readfirstlane(__hip_atomic_load((GAS unsigned*)(ws + Z_TMO), RLX_AGENT)) != 0u;
  return false;
}
template <int N> DI void gather(unsigned char* ws, size_t off, size_t stride, bool active, float (&v)[N]) {
  for (unsigned spins = 0;;) {
    bool ok = true;
    if (active) {
#pragma unroll
      for (int k = 0; k < N; ++k) {
        const u64 x = ld_granule(ws, off + k * stride);
        v[k] = asf((unsigned)x);
        ok &= (x >> 32) == 1ull;
      }
    }
    if (__all(ok)) break;
    if (spin_fail(spins, ws)) break;
  }
  CBAR();
}
DI void wait_flags64(unsigned char* ws, size_t off, int lane) {
  for (unsigned spins = 0;;) {
    const unsigned f = __hip_atomic_load((GAS unsigned*)(ws + off) + lane, RLX_AGENT);
    if (__all(f != 0u)) break;
    if (spin_fail(spins, ws)) break;
  }
  CBAR();
}

DI void fma_row(float (&acc)[12], float p, const u32x4& a, const u32x2& b) {
#pragma unroll
  for (int k = 0; k < 4; ++k) { acc[2 * k] = fmaf(p, bflo(a[k]), acc[2 * k]); acc[2 * k + 1] = fmaf(p, bfhi(a[k]), acc[2 * k + 1]); }
#pragma unroll
  for (int k = 0; k < 2; ++k) { acc[8 + 2 * k] = fmaf(p, bflo(b[k]), acc[8 + 2 * k]); acc[8 + 2 * k + 1] = fmaf(p, bfhi(b[k]), acc[8 + 2 * k + 1]); }
}
DI void rows_to_lds(float (&acc)[12], LAS float* dst, int lane) {
  const int r = lane >> 4, c = lane & 15;
#pragma unroll
  for (int k = 0; k < 12; ++k) { float a = acc[k]; a += __shfl_xor(a, 16); a += __shfl_xor(a, 32); acc[k] = a; }
  if (r == 0) {
#pragma unroll
    for (int k = 0; k < 8; ++k) dst[8 * c + k] = acc[k];
#pragma unroll
    for (int k = 0; k < 4; ++k) dst[128 + 4 * c + k] = acc[8 + k];
  }
}

DI void out_reduce(unsigned char* ws, u16* __restrict__ out, LAS unsigned char* L, int mat, int col0, int tid) {
  const int col = tid & 127, qd = tid >> 7;
  float v[8];
  gather<8>(ws, Z_GOUT + (((size_t)mat * NH + qd * 8) * DM + col0 + col) * 8, (size_t)DM * 8, true, v);
  LAS float* red = (LAS float*)(L + L_RED);
  red[tid] = ((v[0] + v[1]) + (v[2] + v[3])) + ((v[4] + v[5]) + (v[6] + v[7]));
  WGBAR();
  if (tid < 128) out[mat * DM + col0 + tid] = f2bf((red[tid] + red[128 + tid]) + (red[256 + tid] + red[384 + tid]));
}

constexpr int NQ = 5;
struct QLoads { u32x4 a[8]; u32x2 b[8]; float x[8]; };
DI void q_issue(QLoads& Q, const u16* __restrict__ xA, const u16* __restrict__ wqA, const u16* __restrict__ xB,
                const u16* __restrict__ wqB, int h, int jj, int wave, int lane) {
  const int r = lane >> 4, c = lane & 15;
  const u16* x = wave < 4 ? xA : xB;
  const u16* w = wave < 4 ? wqA : wqB;
#pragma unroll
  for (int i = 0; i < 8; ++i) {
    const int row = 128 * jj + 32 * (wave & 3) + 4 * i + r;
    const u16* p = w + (size_t)row * HD + h * DH;
    Q.a[i] = ntload<u32x4>(p + 8 * c); Q.b[i] = ntload<u32x2>(p + 128 + 4 * c);
    Q.x[i] = bflo((unsigned)x[row]);
  }
}
DI void q_partial(QLoads& Q, LAS unsigned char* L, int wave, int lane) {
  float acc[12];
#pragma unroll
  for (int k = 0; k < 12; ++k) acc[k] = 0.f;
#pragma unroll
  for (int i = 0; i < 8; ++i) fma_row(acc, Q.x[i], Q.a[i], Q.b[i]);
  rows_to_lds(acc, (LAS float*)(L + L_RED) + wave * DH, lane);
}
DI void q_publish(unsigned char* ws, LAS unsigned char* L, int h, int jj, int wave, int lane) {
  const int tid = wave * 64 + lane;
  const LAS float* red = (const LAS float*)(L + L_RED);
  if (tid < 384) {
    const int mt = tid >= DH ? 1 : 0, d = tid - mt * DH;
    const LAS float* rp = red + mt * 4 * DH + d;
    st_granule(ws, Z_GQP + (((size_t)h * NQ + jj) * 384 + tid) * 8, (rp[0] + rp[DH]) + (rp[2 * DH] + rp[3 * DH]));
  }
}
DI void q_gather(unsigned char* ws, LAS unsigned char* L, int h, int part, int wave, int lane) {
  const int tid = wave * 64 + lane;
  if (wave < 3) {
    float v[NQ];
    gather<NQ>(ws, Z_GQP + ((size_t)h * NQ * 384 + part * DH + tid) * 8, 384 * 8, true, v);
    ((LAS u16*)(L + L_QS))[tid] = f2bf(((v[0] + v[1]) + (v[2] + v[3])) + v[4]);
  }
}

DI void a_role(const u16* __restrict__ xA, const u16* __restrict__ wqA, const u16* __restrict__ xB, const u16* __restrict__ wqB,
               const u16* __restrict__ K, const u16* __restrict__ V, const u16* __restrict__ woA,
               unsigned char* ws, u16* __restrict__ out, LAS unsigned char* L, int h, int j, int wave, int lane) {
  const int r = lane >> 4, c = lane & 15, tid = wave * 64 + lane;
  STAMP(0);
  QLoads Q;
  if (j < NQ) q_issue(Q, xA, wqA, xB, wqB, h, j, wave, lane);
  constexpr int KR = A_KR, NB = A_NB, TK = 4 * KR, NTH = SK / TK;
  const int gw = j * 8 + wave;
  const int tb = (NTH * gw) / 48, te = (NTH * (gw + 1)) / 48, NT = te - tb;
  const size_t base = ((size_t)h * SK + tb * TK + r) * DH;
  const u16* kp = K + base;
  const u16* vp = V + base;
  u32x4 ka[NB][KR], va[NB][KR]; u32x2 kb[NB][KR], vb[NB][KR];
#define LOADT(buf, t) do { _Pragma("unroll") for (int i = 0; i < KR; ++i) { \
    const u16* kr = kp + (size_t)((t) * TK + i * 4) * DH; const u16* vr = vp + (size_t)((t) * TK + i * 4) * DH; \
    ka[buf][i] = ntload<u32x4>(kr + 8 * c); kb[buf][i] = ntload<u32x2>(kr + 128 + 4 * c); \
    va[buf][i] = ntload<u32x4>(vr + 8 * c); vb[buf][i] = ntload<u32x2>(vr + 128 + 4 * c); } } while (0)
  if (j < NQ) q_partial(Q, L, wave, lane);
  STAMP(10);
  WGBAR();
  STAMP(11);
  __builtin_amdgcn_sched_barrier(0);
  if (wave >= EARLY_W) {
#pragma unroll
    for (int b = 0; b < NB; ++b) LOADT(b, b);
  }
  __builtin_amdgcn_sched_barrier(0);
  if (j < NQ) q_publish(ws, L, h, j, wave, lane);
  q_gather(ws, L, h, 0, wave, lane);
  STAMP(12);
  __builtin_amdgcn_sched_barrier(0);
  if (wave < EARLY_W) {
#pragma unroll
    for (int b = 0; b < NB; ++b) LOADT(b, b);
  }
  __builtin_amdgcn_sched_barrier(0);
  WGBAR();
  STAMP(1);
  const u32x4 qa = *(const LAS u32x4*)(L + L_QS + 16 * c);
  const u32x2 qb = *(const LAS u32x2*)(L + L_QS + 256 + 8 * c);
  float m = -INFINITY, l = 0.f, acc[12];
#pragma unroll
  for (int k = 0; k < 12; ++k) acc[k] = 0.f;
  for (int t0 = 0; t0 < NT; t0 += NB) {
#pragma unroll
    for (int b = 0; b < NB; ++b) {
      const int t = t0 + b;
      if (t < NT) {
        float s[KR];
        float mn = m;
#pragma unroll
        for (int i = 0; i < KR; ++i) {
          float d = 0.f;
#pragma unroll
          for (int k = 0; k < 4; ++k) d = dot2(ka[b][i][k], qa[k], d);
          d = dot2(kb[b][i][0], qb[0], d); d = dot2(kb[b][i][1], qb[1], d);
          d = row_sum16(d);
          s[i] = rbf(rbf(d) * SCALE);
          mn = fmaxf(mn, s[i]);
        }
        const float alpha = ex2((m - mn) * LOG2E);
        m = mn;
        float ps = 0.f;
#pragma unroll
        for (int i = 0; i < KR; ++i) { s[i] = ex2((s[i] - mn) * LOG2E); ps += s[i]; }
        l = l * alpha + ps;
#pragma unroll
        for (int k = 0; k < 12; ++k) acc[k] *= alpha;
#pragma unroll
        for (int i = 0; i < KR; ++i) fma_row(acc, s[i], va[b][i], vb[b][i]);
        if (t + NB < NT) LOADT(b, t + NB);
      }
    }
  }
#undef LOADT
  STAMP(2);
  u32x4 wv[6];
  if (j < 5) {
#pragma unroll
    for (int i = 0; i < 6; ++i) wv[i] = ntload<u32x4>(woA + (size_t)(h * DH + 24 * wave + 4 * i + r) * DM + 128 * j + 8 * c);
  }
  float M = fmaxf(m, __shfl_xor(m, 16)); M = fmaxf(M, __shfl_xor(M, 32));
  const float f = ex2((m - M) * LOG2E);
  l *= f; l += __shfl_xor(l, 16); l += __shfl_xor(l, 32);
#pragma unroll
  for (int k = 0; k < 12; ++k) acc[k] *= f;
  LAS float* red = (LAS float*)(L + L_RED);
  rows_to_lds(acc, red + wave * 200 + 8, lane);
  if (lane == 0) { red[wave * 200] = M; red[wave * 200 + 1] = l; }
  WGBAR();
  {
    float mw[8], MM = -INFINITY;
#pragma unroll
    for (int w = 0; w < 8; ++w) { mw[w] = red[w * 200]; MM = fmaxf(MM, mw[w]); }
    const int idx = tid < DH ? 8 + tid : 1;
    float s = 0.f;
#pragma unroll
    for (int w = 0; w < 8; ++w) s = fmaf(ex2((mw[w] - MM) * LOG2E), red[w * 200 + idx], s);
    const size_t ga = Z_GAP + ((size_t)h * NA + j) * 200 * 8;
    if (tid < DH) st_granule(ws, ga + (size_t)(8 + tid) * 8, s);
    if (tid == DH) st_granule(ws, ga + 8, s);
    if (tid == DH + 1) st_granule(ws, ga, MM);
  }
  if (j < 5) {
    if (wave < 3) {
      float v[NA], mm[NA], ll[NA];
      const size_t gh = Z_GAP + (size_t)h * NA * 200 * 8;
      for (unsigned spins = 0;;) {
        bool ok = true;
#pragma unroll
        for (int k = 0; k < NA; ++k) {
          const u64 x = ld_granule(ws, gh + (size_t)(k * 200 + 8 + tid) * 8), y = ld_granule(ws, gh + (size_t)(k * 200) * 8),
                    z = ld_granule(ws, gh + (size_t)(k * 200 + 1) * 8);
          v[k] = asf((unsigned)x); mm[k] = asf((unsigned)y); ll[k] = asf((unsigned)z);
          ok &= ((x & y & z) >> 32) == 1ull;
        }
        if (__all(ok)) break;
        if (spin_fail(spins, ws)) break;
      }
      CBAR();
      float Mx = mm[0];
#pragma unroll
      for (int k = 1; k < NA; ++k) Mx = fmaxf(Mx, mm[k]);
      float Lsum = 0.f, o = 0.f;
#pragma unroll
      for (int k = 0; k < NA; ++k) { const float g = ex2((mm[k] - Mx) * LOG2E); Lsum = fmaf(g, ll[k], Lsum); o = fmaf(g, v[k], o); }
      ((LAS float*)(L + L_VEC))[tid] = rbf(o / Lsum);
    }
    WGBAR();
    STAMP(3);
    const LAS float* vec = (const LAS float*)(L + L_VEC);
    float a8[8];
#pragma unroll
    for (int k = 0; k < 8; ++k) a8[k] = 0.f;
#pragma unroll
    for (int i = 0; i < 6; ++i) {
      const float x = vec[24 * wave + 4 * i + r];
#pragma unroll
      for (int k = 0; k < 4; ++k) { a8[2 * k] = fmaf(x, bflo(wv[i][k]), a8[2 * k]); a8[2 * k + 1] = fmaf(x, bfhi(wv[i][k]), a8[2 * k + 1]); }
    }
#pragma unroll
    for (int k = 0; k < 8; ++k) { a8[k] += __shfl_xor(a8[k], 16); a8[k] += __shfl_xor(a8[k], 32); }
    LAS float* r2 = (LAS float*)(L + L_BIG);
    if (r == 0) {
#pragma unroll
      for (int k = 0; k < 8; ++k) r2[wave * 128 + 8 * c + k] = a8[k];
    }
    WGBAR();
    if (tid < 128) {
      float s = 0.f;
#pragma unroll
      for (int w = 0; w < 8; ++w) s += r2[w * 128 + tid];
      st_granule(ws, Z_GOUT + ((size_t)h * DM + 128 * j + tid) * 8, s);
    }
    STAMP(4);
    if (h == 4 * j + 1) out_reduce(ws, out, L, 0, 128 * j, tid);
    STAMP(5);
  }
}

DI void b_role(const u16* __restrict__ xA, const u16* __restrict__ wqA, const u16* __restrict__ xB, const u16* __restrict__ wqB,
               const u16* __restrict__ ckv, const u16* __restrict__ wuk, const u16* __restrict__ wuv, const u16* __restrict__ woB,
               unsigned char* ws, u16* __restrict__ out, LAS unsigned char* L, int h, int half, int wave, int lane) {
  const int r = lane >> 4, c = lane & 15, tid = wave * 64 + lane;
  const int cl = lane & 31, hh = lane >> 5;
  const int kb_ = 2 * h + half;
  const __amdgpu_buffer_rsrc_t rs = __builtin_amdgcn_make_buffer_rsrc((void*)ws, 0, (int)W_END, 0x00020000);
  STAMP(0);
  u32x4 cf[4][6];
  {
    u32x4 ua[12]; u32x2 ub[12];
#define LOADU() do { _Pragma("unroll") for (int i = 0; i < 12; ++i) { \
      const u16* row = wuk + ((size_t)(384 * half + 48 * wave + 4 * i + r) * NH + h) * DH; \
      ua[i] = ntload<u32x4>(row + 8 * c); ub[i] = ntload<u32x2>(row + 128 + 4 * c); } } while (0)
#define LOADC() do { _Pragma("unroll") for (int t = 0; t < 4; ++t) _Pragma("unroll") for (int i = 0; i < 6; ++i) \
        cf[t][i] = ntload<u32x4>(ckv + (size_t)(128 * kb_ + 32 * t) * DC + (size_t)(384 * wave + 64 * i + lane) * 8); } while (0)
    LOADU();
    __builtin_amdgcn_sched_barrier(0);
    if (wave >= 3) LOADC();
    __builtin_amdgcn_sched_barrier(0);
    q_gather(ws, L, h, 1, wave, lane);
    STAMP(15);
    __builtin_amdgcn_sched_barrier(0);
    if (wave == 2) LOADC();
    __builtin_amdgcn_sched_barrier(0);
    WGBAR();
    STAMP(1);
    const u32x4 qa = *(const LAS u32x4*)(L + L_QS + 16 * c);
    const u32x2 qb = *(const LAS u32x2*)(L + L_QS + 256 + 8 * c);
    LAS float* qv = (LAS float*)(L + L_VEC);
#pragma unroll
    for (int i = 0; i < 12; ++i) {
      float s = 0.f;
#pragma unroll
      for (int k = 0; k < 4; ++k) s = dot2(ua[i][k], qa[k], s);
      s = dot2(ub[i][0], qb[0], s); s = dot2(ub[i][1], qb[1], s);
      s = row_sum16(s);
      if (c == 0) qv[48 * wave + 4 * i + r] = s;
    }
    WGBAR();
    if (wave < 2) {
      if (lane < 48) {
        u32x4 o;
#pragma unroll
        for (int k = 0; k < 4; ++k) {
          const float a = qv[8 * lane + 2 * k], b = qv[8 * lane + 2 * k + 1];
          const float ah = rbf(a), bh = rbf(b);
          o[k] = wave ? pack2(a - ah, b - bh) : pack2(ah, bh);
        }
        const int c8 = 48 * half + lane;
        __builtin_amdgcn_raw_buffer_store_b128(o, rs, (int)(wave ? W_QL : W_QH) + ((c8 >> 1) * 64 + h + 32 * (c8 & 1)) * 16, 0, 16);
      }
      VM0();
      if (lane == 0) __hip_atomic_store((GAS unsigned*)(ws + Z_FQABS) + 2 * kb_ + wave, 1u, RLX_AGENT);
      __builtin_amdgcn_sched_barrier(0);
      LOADC();
    }
#undef LOADC
#undef LOADU
  }
  STAMP(2);
#pragma unroll
  for (int rd = 0; rd < 2; ++rd) {
#pragma unroll
    for (int tt = 0; tt < 2; ++tt)
#pragma unroll
      for (int i = 0; i < 6; ++i) {
        const int e = 384 * wave + 64 * i + lane;
        *(LAS u32x4*)(L + L_BIG + tt * 49664 + (e / 96) * 1552 + (e % 96) * 16) = cf[2 * rd + tt][i];
      }
    WGBAR();
#pragma unroll
    for (int tt = 0; tt < 2; ++tt)
#pragma unroll
      for (int s = 0; s < 6; ++s)
        cf[2 * rd + tt][s] = *(const LAS u32x4*)(L + L_BIG + tt * 49664 + cl * 1552 + (96 * wave + 16 * s + 8 * hh) * 2);
    WGBAR();
  }
  {
    for (unsigned spins = 0;;) {
      const u32x2 f = __builtin_amdgcn_raw_buffer_load_b64(rs, (int)Z_FQABS + lane * 8, 0, 16);
      if (__all((f[0] != 0u) & (f[1] != 0u))) break;
      if (spin_fail(spins, ws)) break;
    }
    CBAR();
    STAMP(3);
    u32x4 qh[6], ql[6];
#pragma unroll
    for (int s = 0; s < 6; ++s) {
      qh[s] = __builtin_amdgcn_raw_buffer_load_b128(rs, (int)W_QH + ((wave * 6 + s) * 64 + lane) * 16, 0, 16);
      ql[s] = __builtin_amdgcn_raw_buffer_load_b128(rs, (int)W_QL + ((wave * 6 + s) * 64 + lane) * 16, 0, 16);
    }
    __builtin_amdgcn_sched_barrier(0);
    f32x16 zero;
#pragma unroll
    for (int i = 0; i < 16; ++i) zero[i] = 0.f;
    LAS f32x4* sp = (LAS f32x4*)(L + L_BIG);
#pragma unroll
    for (int t = 0; t < 4; ++t) {
      f32x16 S = zero;
#pragma unroll
      for (int s = 0; s < 6; ++s) S = MFMA32(cf[t][s], ql[s], S);
#pragma unroll
      for (int s = 0; s < 6; ++s) S = MFMA32(cf[t][s], qh[s], S);
#pragma unroll
      for (int g = 0; g < 4; ++g) sp[((wave * 4 + t) * 4 + g) * 64 + lane] = f32x4{S[4 * g], S[4 * g + 1], S[4 * g + 2], S[4 * g + 3]};
    }
    STAMP(4);
    WGBAR();
    float sv[8];
    {
      const int t = wave >> 1;
#pragma unroll
      for (int gg = 0; gg < 2; ++gg) {
        const int g = 2 * (wave & 1) + gg;
        f32x4 a = sp[((0 * 4 + t) * 4 + g) * 64 + lane];
#pragma unroll
        for (int w = 1; w < 8; ++w) a += sp[((w * 4 + t) * 4 + g) * 64 + lane];
#pragma unroll
        for (int k = 0; k < 4; ++k) sv[4 * gg + k] = rbf(rbf(a[k]) * SCALE);
      }
    }
    float m = fmaxf(fmaxf(fmaxf(sv[0], sv[1]), fmaxf(sv[2], sv[3])), fmaxf(fmaxf(sv[4], sv[5]), fmaxf(sv[6], sv[7])));
    m = fmaxf(m, __shfl_xor(m, 32));
    LAS float* mx = (LAS float*)(L + L_RED);
    if (lane < 32) mx[wave * 32 + lane] = m;
    WGBAR();
#pragma unroll
    for (int w = 0; w < 8; ++w) m = fmaxf(m, mx[w * 32 + cl]);
    float l = 0.f;
#pragma unroll
    for (int k = 0; k < 8; ++k) { sv[k] = ex2((sv[k] - m) * LOG2E); l += sv[k]; }
    l += __shfl_xor(l, 32);
    if (lane < 32) mx[256 + wave * 32 + lane] = l;
    LAS u32x4* pf = (LAS u32x4*)(L + L_SF);
    pf[wave * 64 + lane] = u32x4{pack2(sv[0], sv[1]), pack2(sv[2], sv[3]), pack2(sv[4], sv[5]), pack2(sv[6], sv[7])};
    WGBAR();
    u32x4 pb[4][2];
#pragma unroll
    for (int t = 0; t < 4; ++t) { pb[t][0] = pf[(2 * t) * 64 + lane]; pb[t][1] = pf[(2 * t + 1) * 64 + lane]; }
    if (wave == 0 && lane < 32) {
      float ls = 0.f;
#pragma unroll
      for (int w = 0; w < 8; ++w) ls += mx[256 + w * 32 + lane];
      const u32x2 ml = {asu(m), asu(ls)};
      __builtin_amdgcn_raw_buffer_store_b64(ml, rs, (int)W_BML + ((cl * NSB + kb_) * 2) * 4, 0, 16);
    }
    u32x4 idlo, idhi;
#pragma unroll
    for (int k = 0; k < 4; ++k) {
      const int k0 = 8 * hh + 2 * k, k1 = k0 + 1;
      idlo[k] = (cl == k0 ? 0x3f80u : 0u) | (cl == k1 ? 0x3f800000u : 0u);
      idhi[k] = (cl == k0 + 16 ? 0x3f80u : 0u) | (cl == k1 + 16 ? 0x3f800000u : 0u);
    }
    STAMP(5);
    LAS float* ost = (LAS float*)(L + L_BIG) + wave * 3200;
#pragma unroll
    for (int ct = 0; ct < 3; ++ct) {
      f32x16 O = zero;
#pragma unroll
      for (int t = 0; t < 4; ++t) {
        f32x16 T = MFMA32(cf[t][2 * ct], idlo, zero);
        T = MFMA32(cf[t][2 * ct + 1], idhi, T);
        O = MFMA32(pack8(T, 0), pb[t][0], O);
        O = MFMA32(pack8(T, 1), pb[t][1], O);
      }
#pragma unroll
      for (int g = 0; g < 4; ++g)
        *(LAS f32x4*)(ost + cl * 100 + 32 * ct + 8 * g + 4 * hh) = f32x4{O[4 * g], O[4 * g + 1], O[4 * g + 2], O[4 * g + 3]};
    }
    asm volatile("s_waitcnt lgkmcnt(0)" ::: "memory");
#pragma unroll
    for (int i = 0; i < 12; ++i) {
      const int e = i * 64 + lane, hd = e / 24, q4 = e % 24;
      const f32x4 v = *(const LAS f32x4*)(ost + hd * 100 + 4 * q4);
      __builtin_amdgcn_raw_buffer_store_b128(__builtin_bit_cast(u32x4, v), rs, (int)W_BO + ((kb_ * NH + hd) * DC + 96 * wave + 4 * q4) * 4, 0, 16);
    }
    STAMP(6);
    VM0();
    WGBAR();
    if (tid == 0) __hip_atomic_store((GAS unsigned*)(ws + Z_FBATT) + kb_, 1u, RLX_AGENT);
    STAMP(7);
  }
  {
    const int lane2 = opaque(lane);
    const int r = lane2 >> 4, c = lane2 & 15, tid = wave * 64 + lane2;
    if (wave != 0) {
      __builtin_amdgcn_sched_barrier(0);
    }
    u32x4 va[12]; u32x2 vb[12];
#pragma unroll
    for (int i = 0; i < 12; ++i) {
      const u16* row = wuv + ((size_t)(384 * half + 48 * wave + 4 * i + r) * NH + h) * DH;
      va[i] = ntload<u32x4>(row + 8 * c); vb[i] = ntload<u32x2>(row + 128 + 4 * c);
    }
    __builtin_amdgcn_sched_barrier(0);
    wait_flags64(ws, Z_FBATT, lane2);
    STAMP(8);
    const u32x2 st = __builtin_amdgcn_raw_buffer_load_b64(rs, (int)W_BML + (h * NSB + lane2) * 8, 0, 16);
    f32x4 pa[8], pc[8];
    const int l32 = lane2 & 31;
#pragma unroll
    for (int i = 0; i < 8; ++i) {
      const int o = (int)W_BO + (((8 * wave + i) * NH + h) * DC + 384 * half) * 4;
      pa[i] = __builtin_bit_cast(f32x4, __builtin_amdgcn_raw_buffer_load_b128(rs, o + lane2 * 16, 0, 16));
      pc[i] = __builtin_bit_cast(f32x4, __builtin_amdgcn_raw_buffer_load_b128(rs, o + (64 + l32) * 16, 0, 16));
    }
    const float m0 = asf(st[0]), l0 = asf(st[1]);
    float M = m0;
#pragma unroll
    for (int o = 1; o < 64; o <<= 1) M = fmaxf(M, __shfl_xor(M, o));
    const float f0 = ex2((m0 - M) * LOG2E);
    float Ls = f0 * l0;
#pragma unroll
    for (int o = 1; o < 64; o <<= 1) Ls += __shfl_xor(Ls, o);
    const float w0 = f0 / Ls;
    f32x4 sa = {0.f, 0.f, 0.f, 0.f}, sc = {0.f, 0.f, 0.f, 0.f};
#pragma unroll
    for (int i = 0; i < 8; ++i) { const float w = __shfl(w0, 8 * wave + i); sa += w * pa[i]; sc += w * pc[i]; }
    LAS f32x4* acc4 = (LAS f32x4*)(L + L_RED);
    acc4[wave * 96 + lane2] = sa;
    if (lane2 < 32) acc4[wave * 96 + 64 + lane2] = sc;
    __builtin_amdgcn_sched_barrier(0);
    u32x4 wo[24];
    const int lc = lane2 < 40 ? lane2 : 0;
#pragma unroll
    for (int i = 0; i < 24; ++i) wo[i] = ntload<u32x4>(woB + (size_t)(h * DH + 24 * wave + i) * DM + 320 * half + 8 * lc);
    __builtin_amdgcn_sched_barrier(0);
    WGBAR();
    if (tid < 384) {
      const LAS float* af = (const LAS float*)(L + L_RED) + tid;
      float s = 0.f;
#pragma unroll
      for (int w = 0; w < 8; ++w) s += af[w * 384];
      ((LAS float*)(L + L_VEC))[tid] = s;
    }
    WGBAR();
    STAMP(9);
    float acc[12];
#pragma unroll
    for (int k = 0; k < 12; ++k) acc[k] = 0.f;
    const LAS float* cv = (const LAS float*)(L + L_VEC);
#pragma unroll
    for (int i = 0; i < 12; ++i) fma_row(acc, cv[48 * wave + 4 * i + r], va[i], vb[i]);
    LAS float* red = (LAS float*)(L + L_RED);
    rows_to_lds(acc, red + wave * DH, lane2);
    WGBAR();
    if (tid < DH) {
      float s = 0.f;
#pragma unroll
      for (int w = 0; w < 8; ++w) s += red[w * DH + tid];
      st_granule(ws, Z_GCTX + (((size_t)h * 2 + half) * DH + tid) * 8, s);
      float v[1];
      gather<1>(ws, Z_GCTX + (((size_t)h * 2 + (half ^ 1)) * DH + tid) * 8, 0, true, v);
      ((LAS float*)(L + L_VEC))[tid] = rbf(half ? v[0] + s : s + v[0]);
    }
    WGBAR();
    STAMP(10);
    float a8[8];
#pragma unroll
    for (int k = 0; k < 8; ++k) a8[k] = 0.f;
#pragma unroll
    for (int i = 0; i < 24; ++i) {
      const float x = cv[24 * wave + i];
#pragma unroll
      for (int k = 0; k < 4; ++k) { a8[2 * k] = fmaf(x, bflo(wo[i][k]), a8[2 * k]); a8[2 * k + 1] = fmaf(x, bfhi(wo[i][k]), a8[2 * k + 1]); }
    }
    LAS float* r2 = (LAS float*)(L + L_BIG);
    if (lane2 < 40) {
#pragma unroll
      for (int k = 0; k < 8; ++k) r2[wave * 320 + 8 * lane2 + k] = a8[k];
    }
    WGBAR();
    if (tid < 320) {
      float s = 0.f;
#pragma unroll
      for (int w = 0; w < 8; ++w) s += r2[w * 320 + tid];
      st_granule(ws, Z_GOUT + (((size_t)NH + h) * DM + 320 * half + tid) * 8, s);
    }
    STAMP(11);
    if ((kb_ % 12) == 3 && kb_ < 60) out_reduce(ws, out, L, 1, 128 * (kb_ / 12), tid);
    STAMP(12);
  }
}

__global__ void __launch_bounds__(512, 2) mega(const u16* __restrict__ xA, const u16* __restrict__ K, const u16* __restrict__ V,
                                               const u16* __restrict__ wqA, const u16* __restrict__ woA,
                                               const u16* __restrict__ xB, const u16* __restrict__ ckv, const u16* __restrict__ wqB,
                                               const u16* __restrict__ wuk, const u16* __restrict__ wuv, const u16* __restrict__ woB,
                                               unsigned char* ws, u16* __restrict__ out) {
  extern __shared__ __attribute__((aligned(16))) unsigned char smem[];
  LAS unsigned char* L = (LAS unsigned char*)smem;
  const int tid = threadIdx.x, lane = tid & 63;
  const int wave = __builtin_amdgcn_readfirstlane(tid >> 6);
  const int b = blockIdx.x;
  const int h = (b & 7) * 4 + (b >> 6), jj = (b >> 3) & 7;
  if (jj < NA) a_role(xA, wqA, xB, wqB, K, V, woA, ws, out, L, h, jj, wave, lane);
  else b_role(xA, wqA, xB, wqB, ckv, wuk, wuv, woB, ws, out, L, h, jj - NA, wave, lane);
}

__global__ void __launch_bounds__(256) zero_ws(u32x4* ws) {
  const unsigned i = blockIdx.x * 256u + threadIdx.x;
  if (i < (unsigned)(Z_END / 16)) ws[i] = u32x4{0u, 0u, 0u, 0u};
}

extern "C" void kernel_launch(void* const* d_in, const int* in_sizes, int n_in,
                              void* d_out, int out_size, void* d_ws, size_t ws_size,
                              hipStream_t stream) {
  (void)hipFuncSetAttribute((const void*)mega, hipFuncAttributeMaxDynamicSharedMemorySize, L_BYTES);
  (void)hipMemsetAsync(d_ws, 0, Z_END, stream);
  mega<<<256, 512, L_BYTES, stream>>>((const u16*)d_in[0], (const u16*)d_in[1], (const u16*)d_in[2], (const u16*)d_in[3], (const u16*)d_in[4],
                                      (const u16*)d_in[5], (const u16*)d_in[6], (const u16*)d_in[7], (const u16*)d_in[8], (const u16*)d_in[9],
                                      (const u16*)d_in[10], (unsigned char*)d_ws, (u16*)d_out);
}
```

```cpp
#include <hip/hip_runtime.h>
#include <hip/hip_bf16.h>
#include <stdint.h>

typedef unsigned u32x4 __attribute__((ext_vector_type(4)));
typedef unsigned u32x2 __attribute__((ext_vector_type(2)));
typedef __bf16 bf16x2 __attribute__((ext_vector_type(2)));
typedef __bf16 bf16x8 __attribute__((ext_vector_type(8)));
typedef float f32x16 __attribute__((ext_vector_type(16)));
typedef float f32x4 __attribute__((ext_vector_type(4)));
typedef unsigned short u16;
typedef unsigned long long u64;

#define DI __device__ __forceinline__
#define GAS __attribute__((address_space(1)))
#define LAS __attribute__((address_space(3)))
#define RLX_AGENT __ATOMIC_RELAXED, __HIP_MEMORY_SCOPE_AGENT

constexpr int NH = 32, SK = 8192, DH = 192, DM = 640, DC = 768, HD = NH * DH;
constexpr float SCALE = 0.07216878364870322f;
constexpr float LOG2E = 1.4426950408889634f;
#define A_KR 2
#define A_NB 1
#define EARLY_W 3
constexpr int NA = 6;
constexpr int NSB = 64;
constexpr unsigned kSpinCap = 1u << 18;

constexpr size_t Z_GENA = 0, Z_GENB = 128;
constexpr size_t Z_FQABS = 1024;
constexpr size_t Z_FBATT = 2048;
constexpr size_t Z_GRAN = 4096;
constexpr int I_QP = 0;
constexpr int I_CTX = I_QP + NH * 5 * 384;
constexpr int I_AP = I_CTX + NH * 2 * 192;
constexpr int I_OUT = I_AP + NH * NA * 200;
constexpr int I_END = I_OUT + 2 * NH * DM;
constexpr size_t W_QH = Z_GRAN + (size_t)I_END * 16;
constexpr size_t W_QL = W_QH + (size_t)NH * DC * 2;
constexpr size_t W_BML = W_QL + (size_t)NH * DC * 2;
constexpr size_t W_BO = W_BML + (size_t)NH * NSB * 2 * 4;
constexpr size_t W_END = W_BO + (size_t)NSB * NH * DC * 4;
static_assert(W_END < (1ull << 31), "ws layout");

constexpr int L_QS = 0;
constexpr int L_VEC = 512;
constexpr int L_RED = 2048;
constexpr int L_BIG = 16384;
constexpr int L_SF = L_BIG + 131072;
constexpr int L_BYTES = L_SF + 16384;

DI float dot2(unsigned a, unsigned b, float c) {
  return __builtin_amdgcn_fdot2_f32_bf16(__builtin_bit_cast(bf16x2, a), __builtin_bit_cast(bf16x2, b), c, false);
}
DI float rbf(float x) { return (float)(__bf16)x; }
DI float asf(unsigned v) { return __builtin_bit_cast(float, v); }
DI unsigned asu(float v) { return __builtin_bit_cast(unsigned, v); }
DI float bflo(unsigned v) { return __builtin_bit_cast(float, v << 16); }
DI float bfhi(unsigned v) { return __builtin_bit_cast(float, v & 0xffff0000u); }
DI u16 f2bf(float x) { return __builtin_bit_cast(u16, (__bf16)x); }
DI unsigned pack2(float lo, float hi) { return (unsigned)f2bf(lo) | ((unsigned)f2bf(hi) << 16); }
template <int CTRL> DI float dpp(float x) {
  return __builtin_bit_cast(float, __builtin_amdgcn_mov_dpp(__builtin_bit_cast(int, x), CTRL, 0xf, 0xf, true));
}
DI float row_sum16(float x) {
  x += dpp<0x128>(x); x += dpp<0x124>(x); x += dpp<0x122>(x); x += dpp<0x121>(x); return x;
}
DI float ex2(float x) { return __builtin_amdgcn_exp2f(x); }
template <typename T> DI T ntload(const void* p) { return __builtin_nontemporal_load(reinterpret_cast<const T*>(p)); }
#define MFMA32(a, b, c) __builtin_amdgcn_mfma_f32_32x32x16_bf16(__builtin_bit_cast(bf16x8, a), __builtin_bit_cast(bf16x8, b), (c), 0, 0, 0)
DI u32x4 pack8(const f32x16& x, int s) {
  u32x4 p;
#pragma unroll
  for (int j = 0; j < 4; ++j) p[j] = pack2(x[8 * s + 2 * j], x[8 * s + 2 * j + 1]);
  return p;
}
DI int opaque(int x) { asm volatile("" : "+v"(x)); return x; }
#define VM0() asm volatile("s_waitcnt vmcnt(0)" ::: "memory")
#define CBAR() asm volatile("" ::: "memory")
#define WGBAR() do { asm volatile("s_waitcnt lgkmcnt(0)" ::: "memory"); __builtin_amdgcn_s_barrier(); asm volatile("" ::: "memory"); } while (0)

#define STAMP(k) do { } while (0)

typedef __amdgpu_buffer_rsrc_t rsrc_t;
struct Tag { unsigned lo, hi; };
DI Tag load_tag(unsigned char* ws, size_t off) {
  const u64 g = __hip_atomic_load((GAS u64*)(ws + off), RLX_AGENT) + 1ull;
  Tag t;
  t.lo = __builtin_amdgcn_readfirstlane((unsigned)g);
  t.hi = __builtin_amdgcn_readfirstlane((unsigned)(g >> 32));
  return t;
}
DI void st_g(rsrc_t rs, int idx, float v0, float v1, Tag t) {
  const u32x4 x = {asu(v0), asu(v1), t.lo, t.hi};
  __builtin_amdgcn_raw_buffer_store_b128(x, rs, (int)Z_GRAN + idx * 16, 0, 16);
}
DI u32x4 ld_g(rsrc_t rs, int idx) { return __builtin_amdgcn_raw_buffer_load_b128(rs, (int)Z_GRAN + idx * 16, 0, 16); }
DI bool tag_ok(const u32x4& x, Tag t) { return (x[2] == t.lo) & (x[3] == t.hi); }
DI void st_flag(rsrc_t rs, int off, Tag t) {
  const u32x2 x = {t.lo, t.hi};
  __builtin_amdgcn_raw_buffer_store_b64(x, rs, off, 0, 16);
}
DI bool spin_fail(unsigned& spins) {
  __builtin_amdgcn_s_sleep(1);
  return ++spins > kSpinCap;
}
template <int N> DI void gather(rsrc_t rs, int idx, int stride, Tag t, float (&v)[N]) {
  for (unsigned spins = 0;;) {
    bool ok = true;
#pragma unroll
    for (int k = 0; k < N; ++k) {
      const u32x4 x = ld_g(rs, idx + k * stride);
      v[k] = asf(x[0]);
      ok &= tag_ok(x, t);
    }
    if (__all(ok)) break;
    if (spin_fail(spins)) break;
  }
  CBAR();
}

DI void fma_row(float (&acc)[12], float p, const u32x4& a, const u32x2& b) {
#pragma unroll
  for (int k = 0; k < 4; ++k) { acc[2 * k] = fmaf(p, bflo(a[k]), acc[2 * k]); acc[2 * k + 1] = fmaf(p, bfhi(a[k]), acc[2 * k + 1]); }
#pragma unroll
  for (int k = 0; k < 2; ++k) { acc[8 + 2 * k] = fmaf(p, bflo(b[k]), acc[8 + 2 * k]); acc[8 + 2 * k + 1] = fmaf(p, bfhi(b[k]), acc[8 + 2 * k + 1]); }
}
DI void rows_to_lds(float (&acc)[12], LAS float* dst, int lane) {
  const int r = lane >> 4, c = lane & 15;
#pragma unroll
  for (int k = 0; k < 12; ++k) { float a = acc[k]; a += __shfl_xor(a, 16); a += __shfl_xor(a, 32); acc[k] = a; }
  if (r == 0) {
#pragma unroll
    for (int k = 0; k < 8; ++k) dst[8 * c + k] = acc[k];
#pragma unroll
    for (int k = 0; k < 4; ++k) dst[128 + 4 * c + k] = acc[8 + k];
  }
}

DI void out_reduce(rsrc_t rs, unsigned char* ws, u16* __restrict__ out, LAS unsigned char* L, int mat, int col0, int tid, Tag t) {
  const int col = tid & 127, qd = tid >> 7;
  float v[8];
  gather<8>(rs, I_OUT + (mat * NH + qd * 8) * DM + col0 + col, DM, t, v);
  LAS float* red = (LAS float*)(L + L_RED);
  red[tid] = ((v[0] + v[1]) + (v[2] + v[3])) + ((v[4] + v[5]) + (v[6] + v[7]));
  WGBAR();
  if (tid < 128) out[mat * DM + col0 + tid] = f2bf((red[tid] + red[128 + tid]) + (red[256 + tid] + red[384 + tid]));
  if (col0 == 0 && tid == 0) st_flag(rs, (int)(mat ? Z_GENB : Z_GENA), t);
}

constexpr int NQ = 5;
struct QLoads { u32x4 a[8]; u32x2 b[8]; float x[8]; };
DI void q_issue(QLoads& Q, const u16* __restrict__ xA, const u16* __restrict__ wqA, const u16* __restrict__ xB,
                const u16* __restrict__ wqB, int h, int jj, int wave, int lane) {
  const int r = lane >> 4, c = lane & 15;
  const u16* x = wave < 4 ? xA : xB;
  const u16* w = wave < 4 ? wqA : wqB;
#pragma unroll
  for (int i = 0; i < 8; ++i) {
    const int row = 128 * jj + 32 * (wave & 3) + 4 * i + r;
    const u16* p = w + (size_t)row * HD + h * DH;
    Q.a[i] = ntload<u32x4>(p + 8 * c); Q.b[i] = ntload<u32x2>(p + 128 + 4 * c);
    Q.x[i] = bflo((unsigned)x[row]);
  }
}
DI void q_partial(QLoads& Q, LAS unsigned char* L, int wave, int lane) {
  float acc[12];
#pragma unroll
  for (int k = 0; k < 12; ++k) acc[k] = 0.f;
#pragma unroll
  for (int i = 0; i < 8; ++i) fma_row(acc, Q.x[i], Q.a[i], Q.b[i]);
  rows_to_lds(acc, (LAS float*)(L + L_RED) + wave * DH, lane);
}
DI void q_publish(rsrc_t rs, LAS unsigned char* L, int h, int jj, int wave, int lane, Tag ta, Tag tb) {
  const int tid = wave * 64 + lane;
  const LAS float* red = (const LAS float*)(L + L_RED);
  if (wave < 6) {
    const int mt = wave >= 3 ? 1 : 0, d = tid - mt * DH;
    const LAS float* rp = red + mt * 4 * DH + d;
    st_g(rs, I_QP + (h * NQ + jj) * 384 + tid, (rp[0] + rp[DH]) + (rp[2 * DH] + rp[3 * DH]), 0.f, mt ? tb : ta);
  }
}
DI void q_gather(rsrc_t rs, LAS unsigned char* L, int h, int part, int wave, int lane, Tag t) {
  const int tid = wave * 64 + lane;
  if (wave < 3) {
    float v[NQ];
    gather<NQ>(rs, I_QP + h * NQ * 384 + part * DH + tid, 384, t, v);
    ((LAS u16*)(L + L_QS))[tid] = f2bf(((v[0] + v[1]) + (v[2] + v[3])) + v[4]);
  }
}

DI void a_role(const u16* __restrict__ xA, const u16* __restrict__ wqA, const u16* __restrict__ xB, const u16* __restrict__ wqB,
               const u16* __restrict__ K, const u16* __restrict__ V, const u16* __restrict__ woA,
               unsigned char* ws, u16* __restrict__ out, LAS unsigned char* L, int h, int j, int wave, int lane) {
  const int r = lane >> 4, c = lane & 15, tid = wave * 64 + lane;
  STAMP(0);
  const rsrc_t rs = __builtin_amdgcn_make_buffer_rsrc((void*)ws, 0, (int)W_END, 0x00020000);
  const Tag ta = load_tag(ws, Z_GENA), tb = load_tag(ws, Z_GENB);
  QLoads Q;
  if (j < NQ) q_issue(Q, xA, wqA, xB, wqB, h, j, wave, lane);
  constexpr int KR = A_KR, NB = A_NB, TK = 4 * KR, NTH = SK / TK;
  const int gw = j * 8 + wave;
  const int t_beg = (NTH * gw) / 48, t_end = (NTH * (gw + 1)) / 48, NT = t_end - t_beg;
  const size_t base = ((size_t)h * SK + t_beg * TK + r) * DH;
  const u16* kp = K + base;
  const u16* vp = V + base;
  u32x4 ka[NB][KR], va[NB][KR]; u32x2 kb[NB][KR], vb[NB][KR];
#define LOADT(buf, t) do { _Pragma("unroll") for (int i = 0; i < KR; ++i) { \
    const u16* kr = kp + (size_t)((t) * TK + i * 4) * DH; const u16* vr = vp + (size_t)((t) * TK + i * 4) * DH; \
    ka[buf][i] = ntload<u32x4>(kr + 8 * c); kb[buf][i] = ntload<u32x2>(kr + 128 + 4 * c); \
    va[buf][i] = ntload<u32x4>(vr + 8 * c); vb[buf][i] = ntload<u32x2>(vr + 128 + 4 * c); } } while (0)
  if (j < NQ) q_partial(Q, L, wave, lane);
  STAMP(10);
  WGBAR();
  STAMP(11);
  __builtin_amdgcn_sched_barrier(0);
  if (wave >= EARLY_W) {
#pragma unroll
    for (int b = 0; b < NB; ++b) LOADT(b, b);
  }
  __builtin_amdgcn_sched_barrier(0);
  if (j < NQ) q_publish(rs, L, h, j, wave, lane, ta, tb);
  q_gather(rs, L, h, 0, wave, lane, ta);
  STAMP(12);
  __builtin_amdgcn_sched_barrier(0);
  if (wave < EARLY_W) {
#pragma unroll
    for (int b = 0; b < NB; ++b) LOADT(b, b);
  }
  __builtin_amdgcn_sched_barrier(0);
  WGBAR();
  STAMP(1);
  const u32x4 qa = *(const LAS u32x4*)(L + L_QS + 16 * c);
  const u32x2 qb = *(const LAS u32x2*)(L + L_QS + 256 + 8 * c);
  float m = -INFINITY, l = 0.f, acc[12];
#pragma unroll
  for (int k = 0; k < 12; ++k) acc[k] = 0.f;
  for (int t0 = 0; t0 < NT; t0 += NB) {
#pragma unroll
    for (int b = 0; b < NB; ++b) {
      const int t = t0 + b;
      if (t < NT) {
        float s[KR];
        float mn = m;
#pragma unroll
        for (int i = 0; i < KR; ++i) {
          float d = 0.f;
#pragma unroll
          for (int k = 0; k < 4; ++k) d = dot2(ka[b][i][k], qa[k], d);
          d = dot2(kb[b][i][0], qb[0], d); d = dot2(kb[b][i][1], qb[1], d);
          d = row_sum16(d);
          s[i] = rbf(rbf(d) * SCALE);
          mn = fmaxf(mn, s[i]);
        }
        const float alpha = ex2((m - mn) * LOG2E);
        m = mn;
        float ps = 0.f;
#pragma unroll
        for (int i = 0; i < KR; ++i) { s[i] = ex2((s[i] - mn) * LOG2E); ps += s[i]; }
        l = l * alpha + ps;
#pragma unroll
        for (int k = 0; k < 12; ++k) acc[k] *= alpha;
#pragma unroll
        for (int i = 0; i < KR; ++i) fma_row(acc, s[i], va[b][i], vb[b][i]);
        if (t + NB < NT) LOADT(b, t + NB);
      }
    }
  }
#undef LOADT
  STAMP(2);
  u32x4 wv[6];
  if (j < 5) {
#pragma unroll
    for (int i = 0; i < 6; ++i) wv[i] = ntload<u32x4>(woA + (size_t)(h * DH + 24 * wave + 4 * i + r) * DM + 128 * j + 8 * c);
  }
  float M = fmaxf(m, __shfl_xor(m, 16)); M = fmaxf(M, __shfl_xor(M, 32));
  const float f = ex2((m - M) * LOG2E);
  l *= f; l += __shfl_xor(l, 16); l += __shfl_xor(l, 32);
#pragma unroll
  for (int k = 0; k < 12; ++k) acc[k] *= f;
  LAS float* red = (LAS float*)(L + L_RED);
  rows_to_lds(acc, red + wave * 200 + 8, lane);
  if (lane == 0) { red[wave * 200] = M; red[wave * 200 + 1] = l; }
  WGBAR();
  {
    float mw[8], MM = -INFINITY;
#pragma unroll
    for (int w = 0; w < 8; ++w) { mw[w] = red[w * 200]; MM = fmaxf(MM, mw[w]); }
    const int idx = tid < DH ? 8 + tid : 1;
    float s = 0.f;
#pragma unroll
    for (int w = 0; w < 8; ++w) s = fmaf(ex2((mw[w] - MM) * LOG2E), red[w * 200 + idx], s);
    const int ga = I_AP + (h * NA + j) * 200;
    if (tid < DH) st_g(rs, ga + 8 + tid, s, 0.f, ta);
    if (tid == DH) st_g(rs, ga, MM, s, ta);
  }
  if (j < 5) {
    if (wave < 3) {
      float v[NA], mm[NA], ll[NA];
      const int gh = I_AP + h * NA * 200;
      for (unsigned spins = 0;;) {
        bool ok = true;
#pragma unroll
        for (int k = 0; k < NA; ++k) {
          const u32x4 x = ld_g(rs, gh + k * 200 + 8 + tid), y = ld_g(rs, gh + k * 200);
          v[k] = asf(x[0]); mm[k] = asf(y[0]); ll[k] = asf(y[1]);
          ok &= tag_ok(x, ta) & tag_ok(y, ta);
        }
        if (__all(ok)) break;
        if (spin_fail(spins)) break;
      }
      CBAR();
      float Mx = mm[0];
#pragma unroll
      for (int k = 1; k < NA; ++k) Mx = fmaxf(Mx, mm[k]);
      float Lsum = 0.f, o = 0.f;
#pragma unroll
      for (int k = 0; k < NA; ++k) { const float g = ex2((mm[k] - Mx) * LOG2E); Lsum = fmaf(g, ll[k], Lsum); o = fmaf(g, v[k], o); }
      ((LAS float*)(L + L_VEC))[tid] = rbf(o / Lsum);
    }
    WGBAR();
    STAMP(3);
    const LAS float* vec = (const LAS float*)(L + L_VEC);
    float a8[8];
#pragma unroll
    for (int k = 0; k < 8; ++k) a8[k] = 0.f;
#pragma unroll
    for (int i = 0; i < 6; ++i) {
      const float x = vec[24 * wave + 4 * i + r];
#pragma unroll
      for (int k = 0; k < 4; ++k) { a8[2 * k] = fmaf(x, bflo(wv[i][k]), a8[2 * k]); a8[2 * k + 1] = fmaf(x, bfhi(wv[i][k]), a8[2 * k + 1]); }
    }
#pragma unroll
    for (int k = 0; k < 8; ++k) { a8[k] += __shfl_xor(a8[k], 16); a8[k] += __shfl_xor(a8[k], 32); }
    LAS float* r2 = (LAS float*)(L + L_BIG);
    if (r == 0) {
#pragma unroll
      for (int k = 0; k < 8; ++k) r2[wave * 128 + 8 * c + k] = a8[k];
    }
    WGBAR();
    if (tid < 128) {
      float s = 0.f;
#pragma unroll
      for (int w = 0; w < 8; ++w) s += r2[w * 128 + tid];
      st_g(rs, I_OUT + h * DM + 128 * j + tid, s, 0.f, ta);
    }
    STAMP(4);
    if (h == 4 * j + 1) out_reduce(rs, ws, out, L, 0, 128 * j, tid, ta);
    STAMP(5);
  }
}

DI void b_role(const u16* __restrict__ xA, const u16* __restrict__ wqA, const u16* __restrict__ xB, const u16* __restrict__ wqB,
               const u16* __restrict__ ckv, const u16* __restrict__ wuk, const u16* __restrict__ wuv, const u16* __restrict__ woB,
               unsigned char* ws, u16* __restrict__ out, LAS unsigned char* L, int h, int half, int wave, int lane) {
  const int r = lane >> 4, c = lane & 15, tid = wave * 64 + lane;
  const int cl = lane & 31, hh = lane >> 5;
  const int kb_ = 2 * h + half;
  const rsrc_t rs = __builtin_amdgcn_make_buffer_rsrc((void*)ws, 0, (int)W_END, 0x00020000);
  STAMP(0);
  const Tag tb = load_tag(ws, Z_GENB);
  u32x4 cf[4][6];
  {
    u32x4 ua[12]; u32x2 ub[12];
#define LOADU() do { _Pragma("unroll") for (int i = 0; i < 12; ++i) { \
      const u16* row = wuk + ((size_t)(384 * half + 48 * wave + 4 * i + r) * NH + h) * DH; \
      ua[i] = ntload<u32x4>(row + 8 * c); ub[i] = ntload<u32x2>(row + 128 + 4 * c); } } while (0)
#define LOADC() do { _Pragma("unroll") for (int t = 0; t < 4; ++t) _Pragma("unroll") for (int i = 0; i < 6; ++i) \
        cf[t][i] = ntload<u32x4>(ckv + (size_t)(128 * kb_ + 32 * t) * DC + (size_t)(384 * wave + 64 * i + lane) * 8); } while (0)
    LOADU();
    __builtin_amdgcn_sched_barrier(0);
    if (wave >= 3) LOADC();
    __builtin_amdgcn_sched_barrier(0);
    q_gather(rs, L, h, 1, wave, lane, tb);
    STAMP(15);
    __builtin_amdgcn_sched_barrier(0);
    if (wave == 2) LOADC();
    __builtin_amdgcn_sched_barrier(0);
    WGBAR();
    STAMP(1);
    const u32x4 qa = *(const LAS u32x4*)(L + L_QS + 16 * c);
    const u32x2 qb = *(const LAS u32x2*)(L + L_QS + 256 + 8 * c);
    LAS float* qv = (LAS float*)(L + L_VEC);
#pragma unroll
    for (int i = 0; i < 12; ++i) {
      float s = 0.f;
#pragma unroll
      for (int k = 0; k < 4; ++k) s = dot2(ua[i][k], qa[k], s);
      s = dot2(ub[i][0], qb[0], s); s = dot2(ub[i][1], qb[1], s);
      s = row_sum16(s);
      if (c == 0) qv[48 * wave + 4 * i + r] = s;
    }
    WGBAR();
    if (wave < 2) {
      if (lane < 48) {
        u32x4 o;
#pragma unroll
        for (int k = 0; k < 4; ++k) {
          const float a = qv[8 * lane + 2 * k], b = qv[8 * lane + 2 * k + 1];
          const float ah = rbf(a), bh = rbf(b);
          o[k] = wave ? pack2(a - ah, b - bh) : pack2(ah, bh);
        }
        const int c8 = 48 * half + lane;
        __builtin_amdgcn_raw_buffer_store_b128(o, rs, (int)(wave ? W_QL : W_QH) + ((c8 >> 1) * 64 + h + 32 * (c8 & 1)) * 16, 0, 16);
      }
      VM0();
      if (lane == 0) st_flag(rs, (int)Z_FQABS + (2 * kb_ + wave) * 8, tb);
      __builtin_amdgcn_sched_barrier(0);
      LOADC();
    }
#undef LOADC
#undef LOADU
  }
  STAMP(2);
#pragma unroll
  for (int rd = 0; rd < 2; ++rd) {
#pragma unroll
    for (int tt = 0; tt < 2; ++tt)
#pragma unroll
      for (int i = 0; i < 6; ++i) {
        const int e = 384 * wave + 64 * i + lane;
        *(LAS u32x4*)(L + L_BIG + tt * 49664 + (e / 96) * 1552 + (e % 96) * 16) = cf[2 * rd + tt][i];
      }
    WGBAR();
#pragma unroll
    for (int tt = 0; tt < 2; ++tt)
#pragma unroll
      for (int s = 0; s < 6; ++s)
        cf[2 * rd + tt][s] = *(const LAS u32x4*)(L + L_BIG + tt * 49664 + cl * 1552 + (96 * wave + 16 * s + 8 * hh) * 2);
    WGBAR();
  }
  {
    for (unsigned spins = 0;;) {
      const u32x4 f = __builtin_amdgcn_raw_buffer_load_b128(rs, (int)Z_FQABS + lane * 16, 0, 16);
      if (__all((f[0] == tb.lo) & (f[1] == tb.hi) & (f[2] == tb.lo) & (f[3] == tb.hi))) break;
      if (spin_fail(spins)) break;
    }
    CBAR();
    STAMP(3);
    u32x4 qh[6], ql[6];
#pragma unroll
    for (int s = 0; s < 6; ++s) {
      qh[s] = __builtin_amdgcn_raw_buffer_load_b128(rs, (int)W_QH + ((wave * 6 + s) * 64 + lane) * 16, 0, 16);
      ql[s] = __builtin_amdgcn_raw_buffer_load_b128(rs, (int)W_QL + ((wave * 6 + s) * 64 + lane) * 16, 0, 16);
    }
    __builtin_amdgcn_sched_barrier(0);
    f32x16 zero;
#pragma unroll
    for (int i = 0; i < 16; ++i) zero[i] = 0.f;
    LAS f32x4* sp = (LAS f32x4*)(L + L_BIG);
#pragma unroll
    for (int t = 0; t < 4; ++t) {
      f32x16 S = zero;
#pragma unroll
      for (int s = 0; s < 6; ++s) S = MFMA32(cf[t][s], ql[s], S);
#pragma unroll
      for (int s = 0; s < 6; ++s) S = MFMA32(cf[t][s], qh[s], S);
#pragma unroll
      for (int g = 0; g < 4; ++g) sp[((wave * 4 + t) * 4 + g) * 64 + lane] = f32x4{S[4 * g], S[4 * g + 1], S[4 * g + 2], S[4 * g + 3]};
    }
    STAMP(4);
    WGBAR();
    float sv[8];
    {
      const int t = wave >> 1;
#pragma unroll
      for (int gg = 0; gg < 2; ++gg) {
        const int g = 2 * (wave & 1) + gg;
        f32x4 a = sp[((0 * 4 + t) * 4 + g) * 64 + lane];
#pragma unroll
        for (int w = 1; w < 8; ++w) a += sp[((w * 4 + t) * 4 + g) * 64 + lane];
#pragma unroll
        for (int k = 0; k < 4; ++k) sv[4 * gg + k] = rbf(rbf(a[k]) * SCALE);
      }
    }
    float m = fmaxf(fmaxf(fmaxf(sv[0], sv[1]), fmaxf(sv[2], sv[3])), fmaxf(fmaxf(sv[4], sv[5]), fmaxf(sv[6], sv[7])));
    m = fmaxf(m, __shfl_xor(m, 32));
    LAS float* mx = (LAS float*)(L + L_RED);
    if (lane < 32) mx[wave * 32 + lane] = m;
    WGBAR();
#pragma unroll
    for (int w = 0; w < 8; ++w) m = fmaxf(m, mx[w * 32 + cl]);
    float l = 0.f;
#pragma unroll
    for (int k = 0; k < 8; ++k) { sv[k] = ex2((sv[k] - m) * LOG2E); l += sv[k]; }
    l += __shfl_xor(l, 32);
    if (lane < 32) mx[256 + wave * 32 + lane] = l;
    LAS u32x4* pf = (LAS u32x4*)(L + L_SF);
    pf[wave * 64 + lane] = u32x4{pack2(sv[0], sv[1]), pack2(sv[2], sv[3]), pack2(sv[4], sv[5]), pack2(sv[6], sv[7])};
    WGBAR();
    u32x4 pb[4][2];
#pragma unroll
    for (int t = 0; t < 4; ++t) { pb[t][0] = pf[(2 * t) * 64 + lane]; pb[t][1] = pf[(2 * t + 1) * 64 + lane]; }
    if (wave == 0 && lane < 32) {
      float ls = 0.f;
#pragma unroll
      for (int w = 0; w < 8; ++w) ls += mx[256 + w * 32 + lane];
      const u32x2 ml = {asu(m), asu(ls)};
      __builtin_amdgcn_raw_buffer_store_b64(ml, rs, (int)W_BML + ((cl * NSB + kb_) * 2) * 4, 0, 16);
    }
    u32x4 idlo, idhi;
#pragma unroll
    for (int k = 0; k < 4; ++k) {
      const int k0 = 8 * hh + 2 * k, k1 = k0 + 1;
      idlo[k] = (cl == k0 ? 0x3f80u : 0u) | (cl == k1 ? 0x3f800000u : 0u);
      idhi[k] = (cl == k0 + 16 ? 0x3f80u : 0u) | (cl == k1 + 16 ? 0x3f800000u : 0u);
    }
    STAMP(5);
    LAS float* ost = (LAS float*)(L + L_BIG) + wave * 3200;
#pragma unroll
    for (int ct = 0; ct < 3; ++ct) {
      f32x16 O = zero;
#pragma unroll
      for (int t = 0; t < 4; ++t) {
        f32x16 T = MFMA32(cf[t][2 * ct], idlo, zero);
        T = MFMA32(cf[t][2 * ct + 1], idhi, T);
        O = MFMA32(pack8(T, 0), pb[t][0], O);
        O = MFMA32(pack8(T, 1), pb[t][1], O);
      }
#pragma unroll
      for (int g = 0; g < 4; ++g)
        *(LAS f32x4*)(ost + cl * 100 + 32 * ct + 8 * g + 4 * hh) = f32x4{O[4 * g], O[4 * g + 1], O[4 * g + 2], O[4 * g + 3]};
    }
    asm volatile("s_waitcnt lgkmcnt(0)" ::: "memory");
#pragma unroll
    for (int i = 0; i < 12; ++i) {
      const int e = i * 64 + lane, hd = e / 24, q4 = e % 24;
      const f32x4 v = *(const LAS f32x4*)(ost + hd * 100 + 4 * q4);
      __builtin_amdgcn_raw_buffer_store_b128(__builtin_bit_cast(u32x4, v), rs, (int)W_BO + ((kb_ * NH + hd) * DC + 96 * wave + 4 * q4) * 4, 0, 16);
    }
    STAMP(6);
    VM0();
    WGBAR();
    if (tid == 0) st_flag(rs, (int)Z_FBATT + kb_ * 8, tb);
    STAMP(7);
  }
  {
    const int lane2 = opaque(lane);
    const int r = lane2 >> 4, c = lane2 & 15, tid = wave * 64 + lane2;
    if (wave != 0) {
      __builtin_amdgcn_sched_barrier(0);
    }
    u32x4 va[12]; u32x2 vb[12];
#pragma unroll
    for (int i = 0; i < 12; ++i) {
      const u16* row = wuv + ((size_t)(384 * half + 48 * wave + 4 * i + r) * NH + h) * DH;
      va[i] = ntload<u32x4>(row + 8 * c); vb[i] = ntload<u32x2>(row + 128 + 4 * c);
    }
    __builtin_amdgcn_sched_barrier(0);
    for (unsigned spins = 0;;) {
      const u32x2 f = __builtin_amdgcn_raw_buffer_load_b64(rs, (int)Z_FBATT + lane2 * 8, 0, 16);
      if (__all((f[0] == tb.lo) & (f[1] == tb.hi))) break;
      if (spin_fail(spins)) break;
    }
    CBAR();
    STAMP(8);
    const u32x2 st = __builtin_amdgcn_raw_buffer_load_b64(rs, (int)W_BML + (h * NSB + lane2) * 8, 0, 16);
    f32x4 pa[8], pc[8];
    const int l32 = lane2 & 31;
#pragma unroll
    for (int i = 0; i < 8; ++i) {
      const int o = (int)W_BO + (((8 * wave + i) * NH + h) * DC + 384 * half) * 4;
      pa[i] = __builtin_bit_cast(f32x4, __builtin_amdgcn_raw_buffer_load_b128(rs, o + lane2 * 16, 0, 16));
      pc[i] = __builtin_bit_cast(f32x4, __builtin_amdgcn_raw_buffer_load_b128(rs, o + (64 + l32) * 16, 0, 16));
    }
    const float m0 = asf(st[0]), l0 = asf(st[1]);
    float M = m0;
#pragma unroll
    for (int o = 1; o < 64; o <<= 1) M = fmaxf(M, __shfl_xor(M, o));
    const float f0 = ex2((m0 - M) * LOG2E);
    float Ls = f0 * l0;
#pragma unroll
    for (int o = 1; o < 64; o <<= 1) Ls += __shfl_xor(Ls, o);
    const float w0 = f0 / Ls;
    f32x4 sa = {0.f, 0.f, 0.f, 0.f}, sc = {0.f, 0.f, 0.f, 0.f};
#pragma unroll
    for (int i = 0; i < 8; ++i) { const float w = __shfl(w0, 8 * wave + i); sa += w * pa[i]; sc += w * pc[i]; }
    LAS f32x4* acc4 = (LAS f32x4*)(L + L_RED);
    acc4[wave * 96 + lane2] = sa;
    if (lane2 < 32) acc4[wave * 96 + 64 + lane2] = sc;
    __builtin_amdgcn_sched_barrier(0);
    u32x4 wo[24];
    const int lc = lane2 < 40 ? lane2 : 0;
#pragma unroll
    for (int i = 0; i < 24; ++i) wo[i] = ntload<u32x4>(woB + (size_t)(h * DH + 24 * wave + i) * DM + 320 * half + 8 * lc);
    __builtin_amdgcn_sched_barrier(0);
    WGBAR();
    if (tid < 384) {
      const LAS float* af = (const LAS float*)(L + L_RED) + tid;
      float s = 0.f;
#pragma unroll
      for (int w = 0; w < 8; ++w) s += af[w * 384];
      ((LAS float*)(L + L_VEC))[tid] = s;
    }
    WGBAR();
    STAMP(9);
    float acc[12];
#pragma unroll
    for (int k = 0; k < 12; ++k) acc[k] = 0.f;
    const LAS float* cv = (const LAS float*)(L + L_VEC);
#pragma unroll
    for (int i = 0; i < 12; ++i) fma_row(acc, cv[48 * wave + 4 * i + r], va[i], vb[i]);
    LAS float* red = (LAS float*)(L + L_RED);
    rows_to_lds(acc, red + wave * DH, lane2);
    WGBAR();
    if (tid < DH) {
      float s = 0.f;
#pragma unroll
      for (int w = 0; w < 8; ++w) s += red[w * DH + tid];
      st_g(rs, I_CTX + (h * 2 + half) * DH + tid, s, 0.f, tb);
      float v[1];
      gather<1>(rs, I_CTX + (h * 2 + (half ^ 1)) * DH + tid, 0, tb, v);
      ((LAS float*)(L + L_VEC))[tid] = rbf(half ? v[0] + s : s + v[0]);
    }
    WGBAR();
    STAMP(10);
    float a8[8];
#pragma unroll
    for (int k = 0; k < 8; ++k) a8[k] = 0.f;
#pragma unroll
    for (int i = 0; i < 24; ++i) {
      const float x = cv[24 * wave + i];
#pragma unroll
      for (int k = 0; k < 4; ++k) { a8[2 * k] = fmaf(x, bflo(wo[i][k]), a8[2 * k]); a8[2 * k + 1] = fmaf(x, bfhi(wo[i][k]), a8[2 * k + 1]); }
    }
    LAS float* r2 = (LAS float*)(L + L_BIG);
    if (lane2 < 40) {
#pragma unroll
      for (int k = 0; k < 8; ++k) r2[wave * 320 + 8 * lane2 + k] = a8[k];
    }
    WGBAR();
    if (tid < 320) {
      float s = 0.f;
#pragma unroll
      for (int w = 0; w < 8; ++w) s += r2[w * 320 + tid];
      st_g(rs, I_OUT + (NH + h) * DM + 320 * half + tid, s, 0.f, tb);
    }
    STAMP(11);
    if ((kb_ % 12) == 3 && kb_ < 60) out_reduce(rs, ws, out, L, 1, 128 * (kb_ / 12), tid, tb);
    STAMP(12);
  }
}

__global__ void __launch_bounds__(512, 2) mega(const u16* __restrict__ xA, const u16* __restrict__ K, const u16* __restrict__ V,
                                               const u16* __restrict__ wqA, const u16* __restrict__ woA,
                                               const u16* __restrict__ xB, const u16* __restrict__ ckv, const u16* __restrict__ wqB,
                                               const u16* __restrict__ wuk, const u16* __restrict__ wuv, const u16* __restrict__ woB,
                                               unsigned char* ws, u16* __restrict__ out) {
  extern __shared__ __attribute__((aligned(16))) unsigned char smem[];
  LAS unsigned char* L = (LAS unsigned char*)smem;
  const int tid = threadIdx.x, lane = tid & 63;
  const int wave = __builtin_amdgcn_readfirstlane(tid >> 6);
  const int b = blockIdx.x;
  const int h = (b & 7) * 4 + (b >> 6), jj = (b >> 3) & 7;
  if (jj < NA) a_role(xA, wqA, xB, wqB, K, V, woA, ws, out, L, h, jj, wave, lane);
  else b_role(xA, wqA, xB, wqB, ckv, wuk, wuv, woB, ws, out, L, h, jj - NA, wave, lane);
}

extern "C" void kernel_launch(void* const* d_in, const int* in_sizes, int n_in,
                              void* d_out, int out_size, void* d_ws, size_t ws_size,
                              hipStream_t stream) {
  (void)hipFuncSetAttribute((const void*)mega, hipFuncAttributeMaxDynamicSharedMemorySize, L_BYTES);
  mega<<<256, 512, L_BYTES, stream>>>((const u16*)d_in[0], (const u16*)d_in[1], (const u16*)d_in[2], (const u16*)d_in[3], (const u16*)d_in[4],
                                      (const u16*)d_in[5], (const u16*)d_in[6], (const u16*)d_in[7], (const u16*)d_in[8], (const u16*)d_in[9],
                                      (const u16*)d_in[10], (unsigned char*)d_ws, (u16*)d_out);
}
```

```cpp
#include <hip/hip_runtime.h>
#include <hip/hip_bf16.h>
#include <stdint.h>

typedef unsigned u32x4 __attribute__((ext_vector_type(4)));
typedef unsigned u32x2 __attribute__((ext_vector_type(2)));
typedef __bf16 bf16x2 __attribute__((ext_vector_type(2)));
typedef __bf16 bf16x8 __attribute__((ext_vector_type(8)));
typedef float f32x16 __attribute__((ext_vector_type(16)));
typedef float f32x4 __attribute__((ext_vector_type(4)));
typedef unsigned short u16;
typedef unsigned long long u64;

#define DI __device__ __forceinline__
#define GAS __attribute__((address_space(1)))
#define LAS __attribute__((address_space(3)))
#define RLX_AGENT __ATOMIC_RELAXED, __HIP_MEMORY_SCOPE_AGENT

constexpr int NH = 32, SK = 8192, DH = 192, DM = 640, DC = 768, HD = NH * DH;
constexpr float SCALE = 0.07216878364870322f;
constexpr float LOG2E = 1.4426950408889634f;
#define A_KR 2
#define A_NB 1
#define EARLY_W 3
constexpr int NA = 6;
constexpr int NSB = 64;
constexpr unsigned kSpinCap = 1u << 18;

constexpr size_t Z_GENA = 0, Z_GENB = 128;
constexpr size_t Z_FQABS = 1024;
constexpr size_t Z_FBATT = 2048;
constexpr size_t Z_GRAN = 4096;
constexpr int I_QP = 0;
constexpr int I_CTX = I_QP + NH * 5 * 384;
constexpr int I_AP = I_CTX + NH * 2 * 192;
constexpr int I_OUT = I_AP + NH * NA * 200;
constexpr int I_END = I_OUT + 2 * NH * DM;
constexpr size_t W_QH = Z_GRAN + (size_t)I_END * 16;
constexpr size_t W_QL = W_QH + (size_t)NH * DC * 2;
constexpr size_t W_BML = W_QL + (size_t)NH * DC * 2;
constexpr size_t W_BO = W_BML + (size_t)NH * NSB * 2 * 4;
constexpr size_t W_END = W_BO + (size_t)NSB * NH * DC * 4;
static_assert(W_END < (1ull << 31), "ws layout");

constexpr int L_QS = 0;
constexpr int L_VEC = 512;
constexpr int L_RED = 2048;
constexpr int L_BIG = 16384;
constexpr int L_SF = L_BIG + 131072;
constexpr int L_BYTES = L_SF + 16384;

DI float dot2(unsigned a, unsigned b, float c) {
  return __builtin_amdgcn_fdot2_f32_bf16(__builtin_bit_cast(bf16x2, a), __builtin_bit_cast(bf16x2, b), c, false);
}
DI float rbf(float x) { return (float)(__bf16)x; }
DI float asf(unsigned v) { return __builtin_bit_cast(float, v); }
DI unsigned asu(float v) { return __builtin_bit_cast(unsigned, v); }
DI float bflo(unsigned v) { return __builtin_bit_cast(float, v << 16); }
DI float bfhi(unsigned v) { return __builtin_bit_cast(float, v & 0xffff0000u); }
DI u16 f2bf(float x) { return __builtin_bit_cast(u16, (__bf16)x); }
DI unsigned pack2(float lo, float hi) { return (unsigned)f2bf(lo) | ((unsigned)f2bf(hi) << 16); }
template <int CTRL> DI float dpp(float x) {
  return __builtin_bit_cast(float, __builtin_amdgcn_mov_dpp(__builtin_bit_cast(int, x), CTRL, 0xf, 0xf, true));
}
DI float row_sum16(float x) {
  x += dpp<0x128>(x); x += dpp<0x124>(x); x += dpp<0x122>(x); x += dpp<0x121>(x); return x;
}
DI float ex2(float x) { return __builtin_amdgcn_exp2f(x); }
template <typename T> DI T ntload(const void* p) { return __builtin_nontemporal_load(reinterpret_cast<const T*>(p)); }
#define MFMA32(a, b, c) __builtin_amdgcn_mfma_f32_32x32x16_bf16(__builtin_bit_cast(bf16x8, a), __builtin_bit_cast(bf16x8, b), (c), 0, 0, 0)
DI u32x4 pack8(const f32x16& x, int s) {
  u32x4 p;
#pragma unroll
  for (int j = 0; j < 4; ++j) p[j] = pack2(x[8 * s + 2 * j], x[8 * s + 2 * j + 1]);
  return p;
}
DI int opaque(int x) { asm volatile("" : "+v"(x)); return x; }
#define VM0() asm volatile("s_waitcnt vmcnt(0)" ::: "memory")
#define CBAR() asm volatile("" ::: "memory")
#define WGBAR() do { asm volatile("s_waitcnt lgkmcnt(0)" ::: "memory"); __builtin_amdgcn_s_barrier(); asm volatile("" ::: "memory"); } while (0)

#define STAMP(k) do { } while (0)

typedef __amdgpu_buffer_rsrc_t rsrc_t;
struct Tag { unsigned lo, hi; };
DI Tag load_tag(unsigned char* ws, size_t off) {
  const u64 g = __hip_atomic_load((GAS u64*)(ws + off), RLX_AGENT) + 1ull;
  Tag t;
  t.lo = __builtin_amdgcn_readfirstlane((unsigned)g);
  t.hi = __builtin_amdgcn_readfirstlane((unsigned)(g >> 32));
  return t;
}
DI void st_g(rsrc_t rs, int idx, float v0, float v1, Tag t) {
  const u32x4 x = {asu(v0), asu(v1), t.lo, t.hi};
  __builtin_amdgcn_raw_buffer_store_b128(x, rs, (int)Z_GRAN + idx * 16, 0, 16);
}
DI u32x4 ld_g(rsrc_t rs, int idx) { return __builtin_amdgcn_raw_buffer_load_b128(rs, (int)Z_GRAN + idx * 16, 0, 16); }
DI bool tag_ok(const u32x4& x, Tag t) { return (x[2] == t.lo) & (x[3] == t.hi); }
DI void st_flag(rsrc_t rs, int off, Tag t) {
  const u32x2 x = {t.lo, t.hi};
  __builtin_amdgcn_raw_buffer_store_b64(x, rs, off, 0, 16);
}
DI bool spin_fail(unsigned& spins) {
  __builtin_amdgcn_s_sleep(1);
  return ++spins > kSpinCap;
}
template <int N> DI void gather(rsrc_t rs, int idx, int stride, Tag t, float (&v)[N]) {
  for (unsigned spins = 0;;) {
    bool ok = true;
#pragma unroll
    for (int k = 0; k < N; ++k) {
      const u32x4 x = ld_g(rs, idx + k * stride);
      v[k] = asf(x[0]);
      ok &= tag_ok(x, t);
    }
    if (__all(ok)) break;
    if (spin_fail(spins)) break;
  }
  CBAR();
}

DI void fma_row(float (&acc)[12], float p, const u32x4& a, const u32x2& b) {
#pragma unroll
  for (int k = 0; k < 4; ++k) { acc[2 * k] = fmaf(p, bflo(a[k]), acc[2 * k]); acc[2 * k + 1] = fmaf(p, bfhi(a[k]), acc[2 * k + 1]); }
#pragma unroll
  for (int k = 0; k < 2; ++k) { acc[8 + 2 * k] = fmaf(p, bflo(b[k]), acc[8 + 2 * k]); acc[8 + 2 * k + 1] = fmaf(p, bfhi(b[k]), acc[8 + 2 * k + 1]); }
}
DI void rows_to_lds(float (&acc)[12], LAS float* dst, int lane) {
  const int r = lane >> 4, c = lane & 15;
#pragma unroll
  for (int k = 0; k < 12; ++k) { float a = acc[k]; a += __shfl_xor(a, 16); a += __shfl_xor(a, 32); acc[k] = a; }
  if (r == 0) {
#pragma unroll
    for (int k = 0; k < 8; ++k) dst[8 * c + k] = acc[k];
#pragma unroll
    for (int k = 0; k < 4; ++k) dst[128 + 4 * c + k] = acc[8 + k];
  }
}

DI void out_reduce(rsrc_t rs, unsigned char* ws, u16* __restrict__ out, LAS unsigned char* L, int mat, int col0, int tid, Tag t) {
  const int col = tid & 127, qd = tid >> 7;
  float v[8];
  gather<8>(rs, I_OUT + (mat * NH + qd * 8) * DM + col0 + col, DM, t, v);
  LAS float* red = (LAS float*)(L + L_RED);
  red[tid] = ((v[0] + v[1]) + (v[2] + v[3])) + ((v[4] + v[5]) + (v[6] + v[7]));
  WGBAR();
  if (tid < 128) out[mat * DM + col0 + tid] = f2bf((red[tid] + red[128 + tid]) + (red[256 + tid] + red[384 + tid]));
  if (col0 == 0 && tid == 0) st_flag(rs, (int)(mat ? Z_GENB : Z_GENA), t);
}

constexpr int NQ = 5;
struct QLoads { u32x4 a[8]; u32x2 b[8]; float x[8]; };
DI void q_issue(QLoads& Q, const u16* __restrict__ xA, const u16* __restrict__ wqA, const u16* __restrict__ xB,
                const u16* __restrict__ wqB, int h, int jj, int wave, int lane) {
  const int r = lane >> 4, c = lane & 15;
  const u16* x = wave < 4 ? xA : xB;
  const u16* w = wave < 4 ? wqA : wqB;
#pragma unroll
  for (int i = 0; i < 8; ++i) {
    const int row = 128 * jj + 32 * (wave & 3) + 4 * i + r;
    const u16* p = w + (size_t)row * HD + h * DH;
    Q.a[i] = ntload<u32x4>(p + 8 * c); Q.b[i] = ntload<u32x2>(p + 128 + 4 * c);
    Q.x[i] = bflo((unsigned)x[row]);
  }
}
DI void q_partial(QLoads& Q, LAS unsigned char* L, int wave, int lane) {
  float acc[12];
#pragma unroll
  for (int k = 0; k < 12; ++k) acc[k] = 0.f;
#pragma unroll
  for (int i = 0; i < 8; ++i) fma_row(acc, Q.x[i], Q.a[i], Q.b[i]);
  rows_to_lds(acc, (LAS float*)(L + L_RED) + wave * DH, lane);
}
DI void q_publish(rsrc_t rs, LAS unsigned char* L, int h, int jj, int wave, int lane, Tag ta, Tag tb) {
  const int tid = wave * 64 + lane;
  const LAS float* red = (const LAS float*)(L + L_RED);
  if (wave < 6) {
    const int mt = wave >= 3 ? 1 : 0, d = tid - mt * DH;
    const LAS float* rp = red + mt * 4 * DH + d;
    st_g(rs, I_QP + (h * NQ + jj) * 384 + tid, (rp[0] + rp[DH]) + (rp[2 * DH] + rp[3 * DH]), 0.f, mt ? tb : ta);
  }
}
DI void q_gather(rsrc_t rs, LAS unsigned char* L, int h, int part, int wave, int lane, Tag t) {
  const int tid = wave * 64 + lane;
  if (wave < 3) {
    float v[NQ];
    gather<NQ>(rs, I_QP + h * NQ * 384 + part * DH + tid, 384, t, v);
    ((LAS u16*)(L + L_QS))[tid] = f2bf(((v[0] + v[1]) + (v[2] + v[3])) + v[4]);
  }
}

DI void a_role(const u16* __restrict__ xA, const u16* __restrict__ wqA, const u16* __restrict__ xB, const u16* __restrict__ wqB,
               const u16* __restrict__ K, const u16* __restrict__ V, const u16* __restrict__ woA,
               unsigned char* ws, u16* __restrict__ out, LAS unsigned char* L, int h, int j, int wave, int lane) {
  const int r = lane >> 4, c = lane & 15, tid = wave * 64 + lane;
  STAMP(0);
  const rsrc_t rs = __builtin_amdgcn_make_buffer_rsrc((void*)ws, 0, (int)W_END, 0x00020000);
  const Tag ta = load_tag(ws, Z_GENA), tb = load_tag(ws, Z_GENB);
  QLoads Q;
  if (j < NQ) q_issue(Q, xA, wqA, xB, wqB, h, j, wave, lane);
  constexpr int KR = A_KR, NB = A_NB, TK = 4 * KR, NTH = SK / TK;
  const int gw = j * 8 + wave;
  const int t_beg = (NTH * gw) / 48, t_end = (NTH * (gw + 1)) / 48, NT = t_end - t_beg;
  const size_t base = ((size_t)h * SK + t_beg * TK + r) * DH;
  const u16* kp = K + base;
  const u16* vp = V + base;
  u32x4 ka[NB][KR], va[NB][KR]; u32x2 kb[NB][KR], vb[NB][KR];
#define LOADT(buf, t) do { _Pragma("unroll") for (int i = 0; i < KR; ++i) { \
    const u16* kr = kp + (size_t)((t) * TK + i * 4) * DH; const u16* vr = vp + (size_t)((t) * TK + i * 4) * DH; \
    ka[buf][i] = ntload<u32x4>(kr + 8 * c); kb[buf][i] = ntload<u32x2>(kr + 128 + 4 * c); \
    va[buf][i] = ntload<u32x4>(vr + 8 * c); vb[buf][i] = ntload<u32x2>(vr + 128 + 4 * c); } } while (0)
  if (j < NQ) q_partial(Q, L, wave, lane);
  STAMP(10);
  WGBAR();
  STAMP(11);
  __builtin_amdgcn_sched_barrier(0);
  if (wave >= EARLY_W) {
#pragma unroll
    for (int b = 0; b < NB; ++b) LOADT(b, b);
  }
  __builtin_amdgcn_sched_barrier(0);
  if (j < NQ) q_publish(rs, L, h, j, wave, lane, ta, tb);
  q_gather(rs, L, h, 0, wave, lane, ta);
  STAMP(12);
  __builtin_amdgcn_sched_barrier(0);
  if (wave < EARLY_W) {
#pragma unroll
    for (int b = 0; b < NB; ++b) LOADT(b, b);
  }
  __builtin_amdgcn_sched_barrier(0);
  WGBAR();
  STAMP(1);
  const u32x4 qa = *(const LAS u32x4*)(L + L_QS + 16 * c);
  const u32x2 qb = *(const LAS u32x2*)(L + L_QS + 256 + 8 * c);
  float m = -INFINITY, l = 0.f, acc[12];
#pragma unroll
  for (int k = 0; k < 12; ++k) acc[k] = 0.f;
  for (int t0 = 0; t0 < NT; t0 += NB) {
#pragma unroll
    for (int b = 0; b < NB; ++b) {
      const int t = t0 + b;
      if (t < NT) {
        float s[KR];
        float mn = m;
#pragma unroll
        for (int i = 0; i < KR; ++i) {
          float d = 0.f;
#pragma unroll
          for (int k = 0; k < 4; ++k) d = dot2(ka[b][i][k], qa[k], d);
          d = dot2(kb[b][i][0], qb[0], d); d = dot2(kb[b][i][1], qb[1], d);
          d = row_sum16(d);
          s[i] = rbf(rbf(d) * SCALE);
          mn = fmaxf(mn, s[i]);
        }
        const float alpha = ex2((m - mn) * LOG2E);
        m = mn;
        float ps = 0.f;
#pragma unroll
        for (int i = 0; i < KR; ++i) { s[i] = ex2((s[i] - mn) * LOG2E); ps += s[i]; }
        l = l * alpha + ps;
#pragma unroll
        for (int k = 0; k < 12; ++k) acc[k] *= alpha;
#pragma unroll
        for (int i = 0; i < KR; ++i) fma_row(acc, s[i], va[b][i], vb[b][i]);
        if (t + NB < NT) LOADT(b, t + NB);
      }
    }
  }
#undef LOADT
  STAMP(2);
  u32x4 wv[6];
  if (j < 5) {
#pragma unroll
    for (int i = 0; i < 6; ++i) wv[i] = ntload<u32x4>(woA + (size_t)(h * DH + 24 * wave + 4 * i + r) * DM + 128 * j + 8 * c);
  }
  float M = fmaxf(m, __shfl_xor(m, 16)); M = fmaxf(M, __shfl_xor(M, 32));
  const float f = ex2((m - M) * LOG2E);
  l *= f; l += __shfl_xor(l, 16); l += __shfl_xor(l, 32);
#pragma unroll
  for (int k = 0; k < 12; ++k) acc[k] *= f;
  LAS float* red = (LAS float*)(L + L_RED);
  rows_to_lds(acc, red + wave * 200 + 8, lane);
  if (lane == 0) { red[wave * 200] = M; red[wave * 200 + 1] = l; }
  WGBAR();
  {
    float mw[8], MM = -INFINITY;
#pragma unroll
    for (int w = 0; w < 8; ++w) { mw[w] = red[w * 200]; MM = fmaxf(MM, mw[w]); }
    const int idx = tid < DH ? 8 + tid : 1;
    float s = 0.f;
#pragma unroll
    for (int w = 0; w < 8; ++w) s = fmaf(ex2((mw[w] - MM) * LOG2E), red[w * 200 + idx], s);
    const int ga = I_AP + (h * NA + j) * 200;
    if (tid < DH) st_g(rs, ga + 8 + tid, s, 0.f, ta);
    if (tid == DH) st_g(rs, ga, MM, s, ta);
  }
  if (j < 5) {
    if (wave < 3) {
      float v[NA], mm[NA], ll[NA];
      const int gh = I_AP + h * NA * 200;
      for (unsigned spins = 0;;) {
        bool ok = true;
#pragma unroll
        for (int k = 0; k < NA; ++k) {
          const u32x4 x = ld_g(rs, gh + k * 200 + 8 + tid), y = ld_g(rs, gh + k * 200);
          v[k] = asf(x[0]); mm[k] = asf(y[0]); ll[k] = asf(y[1]);
          ok &= tag_ok(x, ta) & tag_ok(y, ta);
        }
        if (__all(ok)) break;
        if (spin_fail(spins)) break;
      }
      CBAR();
      float Mx = mm[0];
#pragma unroll
      for (int k = 1; k < NA; ++k) Mx = fmaxf(Mx, mm[k]);
      float Lsum = 0.f, o = 0.f;
#pragma unroll
      for (int k = 0; k < NA; ++k) { const float g = ex2((mm[k] - Mx) * LOG2E); Lsum = fmaf(g, ll[k], Lsum); o = fmaf(g, v[k], o); }
      ((LAS float*)(L + L_VEC))[tid] = rbf(o / Lsum);
    }
    WGBAR();
    STAMP(3);
    const LAS float* vec = (const LAS float*)(L + L_VEC);
    float a8[8];
#pragma unroll
    for (int k = 0; k < 8; ++k) a8[k] = 0.f;
#pragma unroll
    for (int i = 0; i < 6; ++i) {
      const float x = vec[24 * wave + 4 * i + r];
#pragma unroll
      for (int k = 0; k < 4; ++k) { a8[2 * k] = fmaf(x, bflo(wv[i][k]), a8[2 * k]); a8[2 * k + 1] = fmaf(x, bfhi(wv[i][k]), a8[2 * k + 1]); }
    }
#pragma unroll
    for (int k = 0; k < 8; ++k) { a8[k] += __shfl_xor(a8[k], 16); a8[k] += __shfl_xor(a8[k], 32); }
    LAS float* r2 = (LAS float*)(L + L_BIG);
    if (r == 0) {
#pragma unroll
      for (int k = 0; k < 8; ++k) r2[wave * 128 + 8 * c + k] = a8[k];
    }
    WGBAR();
    if (tid < 128) {
      float s = 0.f;
#pragma unroll
      for (int w = 0; w < 8; ++w) s += r2[w * 128 + tid];
      st_g(rs, I_OUT + h * DM + 128 * j + tid, s, 0.f, ta);
    }
    STAMP(4);
    if (h == 4 * j + 1) out_reduce(rs, ws, out, L, 0, 128 * j, tid, ta);
    STAMP(5);
  }
}

DI void b_role(const u16* __restrict__ xA, const u16* __restrict__ wqA, const u16* __restrict__ xB, const u16* __restrict__ wqB,
               const u16* __restrict__ ckv, const u16* __restrict__ wuk, const u16* __restrict__ wuv, const u16* __restrict__ woB,
               unsigned char* ws, u16* __restrict__ out, LAS unsigned char* L, int h, int half, int wave, int lane) {
  const int r = lane >> 4, c = lane & 15, tid = wave * 64 + lane;
  const int cl = lane & 31, hh = lane >> 5;
  const int kb_ = 2 * h + half;
  const rsrc_t rs = __builtin_amdgcn_make_buffer_rsrc((void*)ws, 0, (int)W_END, 0x00020000);
  STAMP(0);
  const Tag tb = load_tag(ws, Z_GENB);
  u32x4 cf[4][6];
  {
    u32x4 ua[12]; u32x2 ub[12];
#define LOADU() do { _Pragma("unroll") for (int i = 0; i < 12; ++i) { \
      const u16* row = wuk + ((size_t)(384 * half + 48 * wave + 4 * i + r) * NH + h) * DH; \
      ua[i] = ntload<u32x4>(row + 8 * c); ub[i] = ntload<u32x2>(row + 128 + 4 * c); } } while (0)
#define LOADC() do { _Pragma("unroll") for (int t = 0; t < 4; ++t) _Pragma("unroll") for (int i = 0; i < 6; ++i) \
        cf[t][i] = ntload<u32x4>(ckv + (size_t)(128 * kb_ + 32 * t) * DC + (size_t)(384 * wave + 64 * i + lane) * 8); } while (0)
    LOADU();
    __builtin_amdgcn_sched_barrier(0);
    if (wave >= 3) LOADC();
    __builtin_amdgcn_sched_barrier(0);
    q_gather(rs, L, h, 1, wave, lane, tb);
    STAMP(15);
    __builtin_amdgcn_sched_barrier(0);
    if (wave == 2) LOADC();
    __builtin_amdgcn_sched_barrier(0);
    WGBAR();
    STAMP(1);
    const u32x4 qa = *(const LAS u32x4*)(L + L_QS + 16 * c);
    const u32x2 qb = *(const LAS u32x2*)(L + L_QS + 256 + 8 * c);
    LAS float* qv = (LAS float*)(L + L_VEC);
#pragma unroll
    for (int i = 0; i < 12; ++i) {
      float s = 0.f;
#pragma unroll
      for (int k = 0; k < 4; ++k) s = dot2(ua[i][k], qa[k], s);
      s = dot2(ub[i][0], qb[0], s); s = dot2(ub[i][1], qb[1], s);
      s = row_sum16(s);
      if (c == 0) qv[48 * wave + 4 * i + r] = s;
    }
    WGBAR();
    if (wave < 2) {
      if (lane < 48) {
        u32x4 o;
#pragma unroll
        for (int k = 0; k < 4; ++k) {
          const float a = qv[8 * lane + 2 * k], b = qv[8 * lane + 2 * k + 1];
          const float ah = rbf(a), bh = rbf(b);
          o[k] = wave ? pack2(a - ah, b - bh) : pack2(ah, bh);
        }
        const int c8 = 48 * half + lane;
        __builtin_amdgcn_raw_buffer_store_b128(o, rs, (int)(wave ? W_QL : W_QH) + ((c8 >> 1) * 64 + h + 32 * (c8 & 1)) * 16, 0, 16);
      }
      VM0();
      if (lane == 0) st_flag(rs, (int)Z_FQABS + (2 * kb_ + wave) * 8, tb);
      __builtin_amdgcn_sched_barrier(0);
      LOADC();
    }
#undef LOADC
#undef LOADU
  }
  STAMP(2);
#pragma unroll
  for (int rd = 0; rd < 2; ++rd) {
#pragma unroll
    for (int tt = 0; tt < 2; ++tt)
#pragma unroll
      for (int i = 0; i < 6; ++i) {
        const int e = 384 * wave + 64 * i + lane;
        *(LAS u32x4*)(L + L_BIG + tt * 49664 + (e / 96) * 1552 + (e % 96) * 16) = cf[2 * rd + tt][i];
      }
    WGBAR();
#pragma unroll
    for (int tt = 0; tt < 2; ++tt)
#pragma unroll
      for (int s = 0; s < 6; ++s)
        cf[2 * rd + tt][s] = *(const LAS u32x4*)(L + L_BIG + tt * 49664 + cl * 1552 + (96 * wave + 16 * s + 8 * hh) * 2);
    WGBAR();
  }
  {
    for (unsigned spins = 0;;) {
      const u32x4 f = __builtin_amdgcn_raw_buffer_load_b128(rs, (int)Z_FQABS + lane * 16, 0, 16);
      if (__all((f[0] == tb.lo) & (f[1] == tb.hi) & (f[2] == tb.lo) & (f[3] == tb.hi))) break;
      if (spin_fail(spins)) break;
    }
    CBAR();
    STAMP(3);
    u32x4 qh[6], ql[6];
#pragma unroll
    for (int s = 0; s < 6; ++s) {
      qh[s] = __builtin_amdgcn_raw_buffer_load_b128(rs, (int)W_QH + ((wave * 6 + s) * 64 + lane) * 16, 0, 16);
      ql[s] = __builtin_amdgcn_raw_buffer_load_b128(rs, (int)W_QL + ((wave * 6 + s) * 64 + lane) * 16, 0, 16);
    }
    __builtin_amdgcn_sched_barrier(0);
    f32x16 zero;
#pragma unroll
    for (int i = 0; i < 16; ++i) zero[i] = 0.f;
    LAS f32x4* sp = (LAS f32x4*)(L + L_BIG);
#pragma unroll
    for (int t = 0; t < 4; ++t) {
      f32x16 S = zero;
#pragma unroll
      for (int s = 0; s < 6; ++s) S = MFMA32(cf[t][s], ql[s], S);
#pragma unroll
      for (int s = 0; s < 6; ++s) S = MFMA32(cf[t][s], qh[s], S);
#pragma unroll
      for (int g = 0; g < 4; ++g) sp[((wave * 4 + t) * 4 + g) * 64 + lane] = f32x4{S[4 * g], S[4 * g + 1], S[4 * g + 2], S[4 * g + 3]};
    }
    STAMP(4);
    WGBAR();
    float sv[8];
    {
      const int t = wave >> 1;
#pragma unroll
      for (int gg = 0; gg < 2; ++gg) {
        const int g = 2 * (wave & 1) + gg;
        f32x4 a = sp[((0 * 4 + t) * 4 + g) * 64 + lane];
#pragma unroll
        for (int w = 1; w < 8; ++w) a += sp[((w * 4 + t) * 4 + g) * 64 + lane];
#pragma unroll
        for (int k = 0; k < 4; ++k) sv[4 * gg + k] = rbf(rbf(a[k]) * SCALE);
      }
    }
    float m = fmaxf(fmaxf(fmaxf(sv[0], sv[1]), fmaxf(sv[2], sv[3])), fmaxf(fmaxf(sv[4], sv[5]), fmaxf(sv[6], sv[7])));
    m = fmaxf(m, __shfl_xor(m, 32));
    LAS float* mx = (LAS float*)(L + L_RED);
    if (lane < 32) mx[wave * 32 + lane] = m;
    WGBAR();
#pragma unroll
    for (int w = 0; w < 8; ++w) m = fmaxf(m, mx[w * 32 + cl]);
    float l = 0.f;
#pragma unroll
    for (int k = 0; k < 8; ++k) { sv[k] = ex2((sv[k] - m) * LOG2E); l += sv[k]; }
    l += __shfl_xor(l, 32);
    if (lane < 32) mx[256 + wave * 32 + lane] = l;
    LAS u32x4* pf = (LAS u32x4*)(L + L_SF);
    pf[wave * 64 + lane] = u32x4{pack2(sv[0], sv[1]), pack2(sv[2], sv[3]), pack2(sv[4], sv[5]), pack2(sv[6], sv[7])};
    WGBAR();
    u32x4 pb[4][2];
#pragma unroll
    for (int t = 0; t < 4; ++t) { pb[t][0] = pf[(2 * t) * 64 + lane]; pb[t][1] = pf[(2 * t + 1) * 64 + lane]; }
    if (wave == 0 && lane < 32) {
      float ls = 0.f;
#pragma unroll
      for (int w = 0; w < 8; ++w) ls += mx[256 + w * 32 + lane];
      const u32x2 ml = {asu(m), asu(ls)};
      __builtin_amdgcn_raw_buffer_store_b64(ml, rs, (int)W_BML + ((cl * NSB + kb_) * 2) * 4, 0, 16);
    }
    u32x4 idlo, idhi;
#pragma unroll
    for (int k = 0; k < 4; ++k) {
      const int k0 = 8 * hh + 2 * k, k1 = k0 + 1;
      idlo[k] = (cl == k0 ? 0x3f80u : 0u) | (cl == k1 ? 0x3f800000u : 0u);
      idhi[k] = (cl == k0 + 16 ? 0x3f80u : 0u) | (cl == k1 + 16 ? 0x3f800000u : 0u);
    }
    STAMP(5);
    LAS float* ost = (LAS float*)(L + L_BIG) + wave * 3200;
#pragma unroll
    for (int ct = 0; ct < 3; ++ct) {
      f32x16 O = zero;
#pragma unroll
      for (int t = 0; t < 4; ++t) {
        f32x16 T = MFMA32(cf[t][2 * ct], idlo, zero);
        T = MFMA32(cf[t][2 * ct + 1], idhi, T);
        O = MFMA32(pack8(T, 0), pb[t][0], O);
        O = MFMA32(pack8(T, 1), pb[t][1], O);
      }
#pragma unroll
      for (int g = 0; g < 4; ++g)
        *(LAS f32x4*)(ost + cl * 100 + 32 * ct + 8 * g + 4 * hh) = f32x4{O[4 * g], O[4 * g + 1], O[4 * g + 2], O[4 * g + 3]};
    }
    asm volatile("s_waitcnt lgkmcnt(0)" ::: "memory");
#pragma unroll
    for (int i = 0; i < 12; ++i) {
      const int e = i * 64 + lane, hd = e / 24, q4 = e % 24;
      const f32x4 v = *(const LAS f32x4*)(ost + hd * 100 + 4 * q4);
      __builtin_amdgcn_raw_buffer_store_b128(__builtin_bit_cast(u32x4, v), rs, (int)W_BO + ((kb_ * NH + hd) * DC + 96 * wave + 4 * q4) * 4, 0, 16);
    }
    STAMP(6);
    VM0();
    WGBAR();
    if (tid == 0) st_flag(rs, (int)Z_FBATT + kb_ * 8, tb);
    STAMP(7);
  }
  {
    const int lane2 = opaque(lane);
    const int r = lane2 >> 4, c = lane2 & 15, tid = wave * 64 + lane2;
    if (wave != 0) {
      __builtin_amdgcn_sched_barrier(0);
    }
    u32x4 va[12]; u32x2 vb[12];
#pragma unroll
    for (int i = 0; i < 12; ++i) {
      const u16* row = wuv + ((size_t)(384 * half + 48 * wave + 4 * i + r) * NH + h) * DH;
      va[i] = ntload<u32x4>(row + 8 * c); vb[i] = ntload<u32x2>(row + 128 + 4 * c);
    }
    __builtin_amdgcn_sched_barrier(0);
    for (unsigned spins = 0;;) {
      const u32x2 f = __builtin_amdgcn_raw_buffer_load_b64(rs, (int)Z_FBATT + lane2 * 8, 0, 16);
      if (__all((f[0] == tb.lo) & (f[1] == tb.hi))) break;
      if (spin_fail(spins)) break;
    }
    CBAR();
    STAMP(8);
    const u32x2 st = __builtin_amdgcn_raw_buffer_load_b64(rs, (int)W_BML + (h * NSB + lane2) * 8, 0, 16);
    f32x4 pa[8], pc[8];
    const int l32 = lane2 & 31;
#pragma unroll
    for (int i = 0; i < 8; ++i) {
      const int o = (int)W_BO + (((8 * wave + i) * NH + h) * DC + 384 * half) * 4;
      pa[i] = __builtin_bit_cast(f32x4, __builtin_amdgcn_raw_buffer_load_b128(rs, o + lane2 * 16, 0, 16));
      pc[i] = __builtin_bit_cast(f32x4, __builtin_amdgcn_raw_buffer_load_b128(rs, o + (64 + l32) * 16, 0, 16));
    }
    const float m0 = asf(st[0]), l0 = asf(st[1]);
    float M = m0;
#pragma unroll
    for (int o = 1; o < 64; o <<= 1) M = fmaxf(M, __shfl_xor(M, o));
    const float f0 = ex2((m0 - M) * LOG2E);
    float Ls = f0 * l0;
#pragma unroll
    for (int o = 1; o < 64; o <<= 1) Ls += __shfl_xor(Ls, o);
    const float w0 = f0 / Ls;
    f32x4 sa = {0.f, 0.f, 0.f, 0.f}, sc = {0.f, 0.f, 0.f, 0.f};
#pragma unroll
    for (int i = 0; i < 8; ++i) { const float w = __shfl(w0, 8 * wave + i); sa += w * pa[i]; sc += w * pc[i]; }
    LAS f32x4* acc4 = (LAS f32x4*)(L + L_RED);
    acc4[wave * 96 + lane2] = sa;
    if (lane2 < 32) acc4[wave * 96 + 64 + lane2] = sc;
    __builtin_amdgcn_sched_barrier(0);
    u32x4 wo[24];
    const int lc = lane2 < 40 ? lane2 : 0;
#pragma unroll
    for (int i = 0; i < 24; ++i) wo[i] = ntload<u32x4>(woB + (size_t)(h * DH + 24 * wave + i) * DM + 320 * half + 8 * lc);
    __builtin_amdgcn_sched_barrier(0);
    WGBAR();
    if (tid < 384) {
      const LAS float* af = (const LAS float*)(L + L_RED) + tid;
      float s = 0.f;
#pragma unroll
      for (int w = 0; w < 8; ++w) s += af[w * 384];
      ((LAS float*)(L + L_VEC))[tid] = s;
    }
    WGBAR();
    STAMP(9);
    float acc[12];
#pragma unroll
    for (int k = 0; k < 12; ++k) acc[k] = 0.f;
    const LAS float* cv = (const LAS float*)(L + L_VEC);
#pragma unroll
    for (int i = 0; i < 12; ++i) fma_row(acc, cv[48 * wave + 4 * i + r], va[i], vb[i]);
    LAS float* red = (LAS float*)(L + L_RED);
    rows_to_lds(acc, red + wave * DH, lane2);
    WGBAR();
    if (tid < DH) {
      float s = 0.f;
#pragma unroll
      for (int w = 0; w < 8; ++w) s += red[w * DH + tid];
      st_g(rs, I_CTX + (h * 2 + half) * DH + tid, s, 0.f, tb);
      float v[1];
      gather<1>(rs, I_CTX + (h * 2 + (half ^ 1)) * DH + tid, 0, tb, v);
      ((LAS float*)(L + L_VEC))[tid] = rbf(half ? v[0] + s : s + v[0]);
    }
    WGBAR();
    STAMP(10);
    float a8[8];
#pragma unroll
    for (int k = 0; k < 8; ++k) a8[k] = 0.f;
#pragma unroll
    for (int i = 0; i < 24; ++i) {
      const float x = cv[24 * wave + i];
#pragma unroll
      for (int k = 0; k < 4; ++k) { a8[2 * k] = fmaf(x, bflo(wo[i][k]), a8[2 * k]); a8[2 * k + 1] = fmaf(x, bfhi(wo[i][k]), a8[2 * k + 1]); }
    }
    LAS float* r2 = (LAS float*)(L + L_BIG);
    if (lane2 < 40) {
#pragma unroll
      for (int k = 0; k < 8; ++k) r2[wave * 320 + 8 * lane2 + k] = a8[k];
    }
    WGBAR();
    if (tid < 320) {
      float s = 0.f;
#pragma unroll
      for (int w = 0; w < 8; ++w) s += r2[w * 320 + tid];
      st_g(rs, I_OUT + (NH + h) * DM + 320 * half + tid, s, 0.f, tb);
    }
    STAMP(11);
    if ((kb_ % 12) == 3 && kb_ < 60) out_reduce(rs, ws, out, L, 1, 128 * (kb_ / 12), tid, tb);
    STAMP(12);
  }
}

__global__ void __launch_bounds__(512, 2) mega(const u16* __restrict__ xA, const u16* __restrict__ K, const u16* __restrict__ V,
                                               const u16* __restrict__ wqA, const u16* __restrict__ woA,
                                               const u16* __restrict__ xB, const u16* __restrict__ ckv, const u16* __restrict__ wqB,
                                               const u16* __restrict__ wuk, const u16* __restrict__ wuv, const u16* __restrict__ woB,
                                               unsigned char* ws, u16* __restrict__ out) {
  extern __shared__ __attribute__((aligned(16))) unsigned char smem[];
  LAS unsigned char* L = (LAS unsigned char*)smem;
  const int tid = threadIdx.x, lane = tid & 63;
  const int wave = __builtin_amdgcn_readfirstlane(tid >> 6);
  const int b = blockIdx.x;
  const int h = (b & 7) * 4 + (b >> 6), jj = (b >> 3) & 7;
  if (jj == NA - 1) {
    const unsigned char* pc = (const unsigned char*)(__builtin_amdgcn_s_getpc() & ~255ull);
    u32x4 w0 = *(const u32x4*)(pc + (wave * 4 + 0) * 1024 + lane * 16), w1 = *(const u32x4*)(pc + (wave * 4 + 1) * 1024 + lane * 16);
    u32x4 w2 = *(const u32x4*)(pc + (wave * 4 + 2) * 1024 + lane * 16);
    u32x4 w3 = wave < 7 ? *(const u32x4*)(pc + (wave * 4 + 3) * 1024 + lane * 16) : w2;
    asm volatile("" :: "v"(w0), "v"(w1), "v"(w2), "v"(w3));
  }
  if (jj < NA) a_role(xA, wqA, xB, wqB, K, V, woA, ws, out, L, h, jj, wave, lane);
  else b_role(xA, wqA, xB, wqB, ckv, wuk, wuv, woB, ws, out, L, h, jj - NA, wave, lane);
}

extern "C" void kernel_launch(void* const* d_in, const int* in_sizes, int n_in,
                              void* d_out, int out_size, void* d_ws, size_t ws_size,
                              hipStream_t stream) {
  (void)hipFuncSetAttribute((const void*)mega, hipFuncAttributeMaxDynamicSharedMemorySize, L_BYTES);
  mega<<<256, 512, L_BYTES, stream>>>((const u16*)d_in[0], (const u16*)d_in[1], (const u16*)d_in[2], (const u16*)d_in[3], (const u16*)d_in[4],
                                      (const u16*)d_in[5], (const u16*)d_in[6], (const u16*)d_in[7], (const u16*)d_in[8], (const u16*)d_in[9],
                                      (const u16*)d_in[10], (unsigned char*)d_ws, (u16*)d_out);
}
```

```cpp
#include <hip/hip_runtime.h>
#include <hip/hip_bf16.h>
#include <stdint.h>

typedef unsigned u32x4 __attribute__((ext_vector_type(4)));
typedef unsigned u32x2 __attribute__((ext_vector_type(2)));
typedef __bf16 bf16x2 __attribute__((ext_vector_type(2)));
typedef __bf16 bf16x8 __attribute__((ext_vector_type(8)));
typedef float f32x16 __attribute__((ext_vector_type(16)));
typedef float f32x4 __attribute__((ext_vector_type(4)));
typedef unsigned short u16;
typedef unsigned long long u64;

#define DI __device__ __forceinline__
#define GAS __attribute__((address_space(1)))
#define LAS __attribute__((address_space(3)))
#define RLX_AGENT __ATOMIC_RELAXED, __HIP_MEMORY_SCOPE_AGENT

constexpr int NH = 32, SK = 8192, DH = 192, DM = 640, DC = 768, HD = NH * DH;
constexpr float SCALE = 0.07216878364870322f;
constexpr float LOG2E = 1.4426950408889634f;
#define A_KR 2
#define A_NB 1
#define EARLY_W 3
constexpr int NA = 6;
constexpr int NSB = 64;
constexpr unsigned kSpinCap = 1u << 18;

constexpr size_t Z_GENA = 0, Z_GENB = 128;
constexpr size_t Z_FQABS = 1024;
constexpr size_t Z_FBATT = 2048;
constexpr size_t Z_GRAN = 4096;
constexpr int I_QP = 0;
constexpr int I_CTX = I_QP + NH * 5 * 384;
constexpr int I_AP = I_CTX + NH * 2 * 192;
constexpr int I_OUT = I_AP + NH * NA * 200;
constexpr int I_END = I_OUT + 2 * NH * DM;
constexpr size_t W_QH = Z_GRAN + (size_t)I_END * 16;
constexpr size_t W_QL = W_QH + (size_t)NH * DC * 2;
constexpr size_t W_BML = W_QL + (size_t)NH * DC * 2;
constexpr size_t W_BO = W_BML + (size_t)NH * NSB * 2 * 4;
constexpr size_t W_END = W_BO + (size_t)NSB * NH * DC * 2;
static_assert(W_END < (1ull << 31), "ws layout");

constexpr int L_QS = 0;
constexpr int L_VEC = 512;
constexpr int L_RED = 2048;
constexpr int L_BIG = 16384;
constexpr int L_SF = L_BIG + 131072;
constexpr int L_BYTES = L_SF + 16384;

DI float dot2(unsigned a, unsigned b, float c) {
  return __builtin_amdgcn_fdot2_f32_bf16(__builtin_bit_cast(bf16x2, a), __builtin_bit_cast(bf16x2, b), c, false);
}
DI float rbf(float x) { return (float)(__bf16)x; }
DI float asf(unsigned v) { return __builtin_bit_cast(float, v); }
DI unsigned asu(float v) { return __builtin_bit_cast(unsigned, v); }
DI float bflo(unsigned v) { return __builtin_bit_cast(float, v << 16); }
DI float bfhi(unsigned v) { return __builtin_bit_cast(float, v & 0xffff0000u); }
DI u16 f2bf(float x) { return __builtin_bit_cast(u16, (__bf16)x); }
DI unsigned pack2(float lo, float hi) { return (unsigned)f2bf(lo) | ((unsigned)f2bf(hi) << 16); }
template <int CTRL> DI float dpp(float x) {
  return __builtin_bit_cast(float, __builtin_amdgcn_mov_dpp(__builtin_bit_cast(int, x), CTRL, 0xf, 0xf, true));
}
DI float row_sum16(float x) {
  x += dpp<0x128>(x); x += dpp<0x124>(x); x += dpp<0x122>(x); x += dpp<0x121>(x); return x;
}
DI float ex2(float x) { return __builtin_amdgcn_exp2f(x); }
template <typename T> DI T ntload(const void* p) { return __builtin_nontemporal_load(reinterpret_cast<const T*>(p)); }
#define MFMA32(a, b, c) __builtin_amdgcn_mfma_f32_32x32x16_bf16(__builtin_bit_cast(bf16x8, a), __builtin_bit_cast(bf16x8, b), (c), 0, 0, 0)
DI u32x4 pack8(const f32x16& x, int s) {
  u32x4 p;
#pragma unroll
  for (int j = 0; j < 4; ++j) p[j] = pack2(x[8 * s + 2 * j], x[8 * s + 2 * j + 1]);
  return p;
}
DI int opaque(int x) { asm volatile("" : "+v"(x)); return x; }
#define VM0() asm volatile("s_waitcnt vmcnt(0)" ::: "memory")
#define CBAR() asm volatile("" ::: "memory")
#define WGBAR() do { asm volatile("s_waitcnt lgkmcnt(0)" ::: "memory"); __builtin_amdgcn_s_barrier(); asm volatile("" ::: "memory"); } while (0)

#define STAMP(k) do { } while (0)

typedef __amdgpu_buffer_rsrc_t rsrc_t;
struct Tag { unsigned lo, hi; };
DI Tag load_tag(unsigned char* ws, size_t off) {
  const u64 g = __hip_atomic_load((GAS u64*)(ws + off), RLX_AGENT) + 1ull;
  Tag t;
  t.lo = __builtin_amdgcn_readfirstlane((unsigned)g);
  t.hi = __builtin_amdgcn_readfirstlane((unsigned)(g >> 32));
  return t;
}
DI void st_g(rsrc_t rs, int idx, float v0, float v1, Tag t) {
  const u32x4 x = {asu(v0), asu(v1), t.lo, t.hi};
  __builtin_amdgcn_raw_buffer_store_b128(x, rs, (int)Z_GRAN + idx * 16, 0, 16);
}
DI u32x4 ld_g(rsrc_t rs, int idx) { return __builtin_amdgcn_raw_buffer_load_b128(rs, (int)Z_GRAN + idx * 16, 0, 16); }
DI bool tag_ok(const u32x4& x, Tag t) { return (x[2] == t.lo) & (x[3] == t.hi); }
DI void st_flag(rsrc_t rs, int off, Tag t) {
  const u32x2 x = {t.lo, t.hi};
  __builtin_amdgcn_raw_buffer_store_b64(x, rs, off, 0, 16);
}
DI bool spin_fail(unsigned& spins) {
  __builtin_amdgcn_s_sleep(1);
  return ++spins > kSpinCap;
}
template <int N> DI void gather(rsrc_t rs, int idx, int stride, Tag t, float (&v)[N]) {
  for (unsigned spins = 0;;) {
    bool ok = true;
#pragma unroll
    for (int k = 0; k < N; ++k) {
      const u32x4 x = ld_g(rs, idx + k * stride);
      v[k] = asf(x[0]);
      ok &= tag_ok(x, t);
    }
    if (__all(ok)) break;
    if (spin_fail(spins)) break;
  }
  CBAR();
}

DI void fma_row(float (&acc)[12], float p, const u32x4& a, const u32x2& b) {
#pragma unroll
  for (int k = 0; k < 4; ++k) { acc[2 * k] = fmaf(p, bflo(a[k]), acc[2 * k]); acc[2 * k + 1] = fmaf(p, bfhi(a[k]), acc[2 * k + 1]); }
#pragma unroll
  for (int k = 0; k < 2; ++k) { acc[8 + 2 * k] = fmaf(p, bflo(b[k]), acc[8 + 2 * k]); acc[8 + 2 * k + 1] = fmaf(p, bfhi(b[k]), acc[8 + 2 * k + 1]); }
}
DI void rows_to_lds(float (&acc)[12], LAS float* dst, int lane) {
  const int r = lane >> 4, c = lane & 15;
#pragma unroll
  for (int k = 0; k < 12; ++k) { float a = acc[k]; a += __shfl_xor(a, 16); a += __shfl_xor(a, 32); acc[k] = a; }
  if (r == 0) {
#pragma unroll
    for (int k = 0; k < 8; ++k) dst[8 * c + k] = acc[k];
#pragma unroll
    for (int k = 0; k < 4; ++k) dst[128 + 4 * c + k] = acc[8 + k];
  }
}

DI void out_reduce(rsrc_t rs, unsigned char* ws, u16* __restrict__ out, LAS unsigned char* L, int mat, int col0, int tid, Tag t) {
  const int col = tid & 127, qd = tid >> 7;
  float v[8];
  gather<8>(rs, I_OUT + (mat * NH + qd * 8) * DM + col0 + col, DM, t, v);
  LAS float* red = (LAS float*)(L + L_RED);
  red[tid] = ((v[0] + v[1]) + (v[2] + v[3])) + ((v[4] + v[5]) + (v[6] + v[7]));
  WGBAR();
  if (tid < 128) out[mat * DM + col0 + tid] = f2bf((red[tid] + red[128 + tid]) + (red[256 + tid] + red[384 + tid]));
  if (col0 == 0 && tid == 0) st_flag(rs, (int)(mat ? Z_GENB : Z_GENA), t);
}

constexpr int NQ = 5;
struct QLoads { u32x4 a[8]; u32x2 b[8]; float x[8]; };
DI void q_issue(QLoads& Q, const u16* __restrict__ xA, const u16* __restrict__ wqA, const u16* __restrict__ xB,
                const u16* __restrict__ wqB, int h, int jj, int wave, int lane) {
  const int r = lane >> 4, c = lane & 15;
  const u16* x = wave < 4 ? xA : xB;
  const u16* w = wave < 4 ? wqA : wqB;
#pragma unroll
  for (int i = 0; i < 8; ++i) {
    const int row = 128 * jj + 32 * (wave & 3) + 4 * i + r;
    const u16* p = w + (size_t)row * HD + h * DH;
    Q.a[i] = ntload<u32x4>(p + 8 * c); Q.b[i] = ntload<u32x2>(p + 128 + 4 * c);
    Q.x[i] = bflo((unsigned)x[row]);
  }
}
DI void q_partial(QLoads& Q, LAS unsigned char* L, int wave, int lane) {
  float acc[12];
#pragma unroll
  for (int k = 0; k < 12; ++k) acc[k] = 0.f;
#pragma unroll
  for (int i = 0; i < 8; ++i) fma_row(acc, Q.x[i], Q.a[i], Q.b[i]);
  rows_to_lds(acc, (LAS float*)(L + L_RED) + wave * DH, lane);
}
DI void q_publish(rsrc_t rs, LAS unsigned char* L, int h, int jj, int wave, int lane, Tag ta, Tag tb) {
  const int tid = wave * 64 + lane;
  const LAS float* red = (const LAS float*)(L + L_RED);
  if (wave < 6) {
    const int mt = wave >= 3 ? 1 : 0, d = tid - mt * DH;
    const LAS float* rp = red + mt * 4 * DH + d;
    st_g(rs, I_QP + (h * NQ + jj) * 384 + tid, (rp[0] + rp[DH]) + (rp[2 * DH] + rp[3 * DH]), 0.f, mt ? tb : ta);
  }
}
DI void q_gather(rsrc_t rs, LAS unsigned char* L, int h, int part, int wave, int lane, Tag t) {
  const int tid = wave * 64 + lane;
  if (wave < 3) {
    float v[NQ];
    gather<NQ>(rs, I_QP + h * NQ * 384 + part * DH + tid, 384, t, v);
    ((LAS u16*)(L + L_QS))[tid] = f2bf(((v[0] + v[1]) + (v[2] + v[3])) + v[4]);
  }
}

DI void a_role(const u16* __restrict__ xA, const u16* __restrict__ wqA, const u16* __restrict__ xB, const u16* __restrict__ wqB,
               const u16* __restrict__ K, const u16* __restrict__ V, const u16* __restrict__ woA,
               unsigned char* ws, u16* __restrict__ out, LAS unsigned char* L, int h, int j, int wave, int lane) {
  const int r = lane >> 4, c = lane & 15, tid = wave * 64 + lane;
  STAMP(0);
  const rsrc_t rs = __builtin_amdgcn_make_buffer_rsrc((void*)ws, 0, (int)W_END, 0x00020000);
  const Tag ta = load_tag(ws, Z_GENA), tb = load_tag(ws, Z_GENB);
  QLoads Q;
  if (j < NQ) q_issue(Q, xA, wqA, xB, wqB, h, j, wave, lane);
  constexpr int KR = A_KR, NB = A_NB, TK = 4 * KR, NTH = SK / TK;
  const int gw = j * 8 + wave;
  const int t_beg = (NTH * gw) / 48, t_end = (NTH * (gw + 1)) / 48, NT = t_end - t_beg;
  const size_t base = ((size_t)h * SK + t_beg * TK + r) * DH;
  const u16* kp = K + base;
  const u16* vp = V + base;
  u32x4 ka[NB][KR], va[NB][KR]; u32x2 kb[NB][KR], vb[NB][KR];
#define LOADT(buf, t) do { _Pragma("unroll") for (int i = 0; i < KR; ++i) { \
    const u16* kr = kp + (size_t)((t) * TK + i * 4) * DH; const u16* vr = vp + (size_t)((t) * TK + i * 4) * DH; \
    ka[buf][i] = ntload<u32x4>(kr + 8 * c); kb[buf][i] = ntload<u32x2>(kr + 128 + 4 * c); \
    va[buf][i] = ntload<u32x4>(vr + 8 * c); vb[buf][i] = ntload<u32x2>(vr + 128 + 4 * c); } } while (0)
  if (j < NQ) q_partial(Q, L, wave, lane);
  STAMP(10);
  WGBAR();
  STAMP(11);
  __builtin_amdgcn_sched_barrier(0);
  if (wave >= EARLY_W) {
#pragma unroll
    for (int b = 0; b < NB; ++b) LOADT(b, b);
  }
  __builtin_amdgcn_sched_barrier(0);
  if (j < NQ) q_publish(rs, L, h, j, wave, lane, ta, tb);
  q_gather(rs, L, h, 0, wave, lane, ta);
  STAMP(12);
  __builtin_amdgcn_sched_barrier(0);
  if (wave < EARLY_W) {
#pragma unroll
    for (int b = 0; b < NB; ++b) LOADT(b, b);
  }
  __builtin_amdgcn_sched_barrier(0);
  WGBAR();
  STAMP(1);
  const u32x4 qa = *(const LAS u32x4*)(L + L_QS + 16 * c);
  const u32x2 qb = *(const LAS u32x2*)(L + L_QS + 256 + 8 * c);
  float m = -INFINITY, l = 0.f, acc[12];
#pragma unroll
  for (int k = 0; k < 12; ++k) acc[k] = 0.f;
  for (int t0 = 0; t0 < NT; t0 += NB) {
#pragma unroll
    for (int b = 0; b < NB; ++b) {
      const int t = t0 + b;
      if (t < NT) {
        float s[KR];
        float mn = m;
#pragma unroll
        for (int i = 0; i < KR; ++i) {
          float d = 0.f;
#pragma unroll
          for (int k = 0; k < 4; ++k) d = dot2(ka[b][i][k], qa[k], d);
          d = dot2(kb[b][i][0], qb[0], d); d = dot2(kb[b][i][1], qb[1], d);
          d = row_sum16(d);
          s[i] = rbf(rbf(d) * SCALE);
          mn = fmaxf(mn, s[i]);
        }
        const float alpha = ex2((m - mn) * LOG2E);
        m = mn;
        float ps = 0.f;
#pragma unroll
        for (int i = 0; i < KR; ++i) { s[i] = ex2((s[i] - mn) * LOG2E); ps += s[i]; }
        l = l * alpha + ps;
#pragma unroll
        for (int k = 0; k < 12; ++k) acc[k] *= alpha;
#pragma unroll
        for (int i = 0; i < KR; ++i) fma_row(acc, s[i], va[b][i], vb[b][i]);
        if (t + NB < NT) LOADT(b, t + NB);
      }
    }
  }
#undef LOADT
  STAMP(2);
  u32x4 wv[6];
  if (j < 5) {
#pragma unroll
    for (int i = 0; i < 6; ++i) wv[i] = ntload<u32x4>(woA + (size_t)(h * DH + 24 * wave + 4 * i + r) * DM + 128 * j + 8 * c);
  }
  float M = fmaxf(m, __shfl_xor(m, 16)); M = fmaxf(M, __shfl_xor(M, 32));
  const float f = ex2((m - M) * LOG2E);
  l *= f; l += __shfl_xor(l, 16); l += __shfl_xor(l, 32);
#pragma unroll
  for (int k = 0; k < 12; ++k) acc[k] *= f;
  LAS float* red = (LAS float*)(L + L_RED);
  rows_to_lds(acc, red + wave * 200 + 8, lane);
  if (lane == 0) { red[wave * 200] = M; red[wave * 200 + 1] = l; }
  WGBAR();
  {
    float mw[8], MM = -INFINITY;
#pragma unroll
    for (int w = 0; w < 8; ++w) { mw[w] = red[w * 200]; MM = fmaxf(MM, mw[w]); }
    const int idx = tid < DH ? 8 + tid : 1;
    float s = 0.f;
#pragma unroll
    for (int w = 0; w < 8; ++w) s = fmaf(ex2((mw[w] - MM) * LOG2E), red[w * 200 + idx], s);
    const int ga = I_AP + (h * NA + j) * 200;
    if (tid < DH) st_g(rs, ga + 8 + tid, s, 0.f, ta);
    if (tid == DH) st_g(rs, ga, MM, s, ta);
  }
  if (j < 5) {
    if (wave < 3) {
      float v[NA], mm[NA], ll[NA];
      const int gh = I_AP + h * NA * 200;
      for (unsigned spins = 0;;) {
        bool ok = true;
#pragma unroll
        for (int k = 0; k < NA; ++k) {
          const u32x4 x = ld_g(rs, gh + k * 200 + 8 + tid), y = ld_g(rs, gh + k * 200);
          v[k] = asf(x[0]); mm[k] = asf(y[0]); ll[k] = asf(y[1]);
          ok &= tag_ok(x, ta) & tag_ok(y, ta);
        }
        if (__all(ok)) break;
        if (spin_fail(spins)) break;
      }
      CBAR();
      float Mx = mm[0];
#pragma unroll
      for (int k = 1; k < NA; ++k) Mx = fmaxf(Mx, mm[k]);
      float Lsum = 0.f, o = 0.f;
#pragma unroll
      for (int k = 0; k < NA; ++k) { const float g = ex2((mm[k] - Mx) * LOG2E); Lsum = fmaf(g, ll[k], Lsum); o = fmaf(g, v[k], o); }
      ((LAS float*)(L + L_VEC))[tid] = rbf(o / Lsum);
    }
    WGBAR();
    STAMP(3);
    const LAS float* vec = (const LAS float*)(L + L_VEC);
    float a8[8];
#pragma unroll
    for (int k = 0; k < 8; ++k) a8[k] = 0.f;
#pragma unroll
    for (int i = 0; i < 6; ++i) {
      const float x = vec[24 * wave + 4 * i + r];
#pragma unroll
      for (int k = 0; k < 4; ++k) { a8[2 * k] = fmaf(x, bflo(wv[i][k]), a8[2 * k]); a8[2 * k + 1] = fmaf(x, bfhi(wv[i][k]), a8[2 * k + 1]); }
    }
#pragma unroll
    for (int k = 0; k < 8; ++k) { a8[k] += __shfl_xor(a8[k], 16); a8[k] += __shfl_xor(a8[k], 32); }
    LAS float* r2 = (LAS float*)(L + L_BIG);
    if (r == 0) {
#pragma unroll
      for (int k = 0; k < 8; ++k) r2[wave * 128 + 8 * c + k] = a8[k];
    }
    WGBAR();
    if (tid < 128) {
      float s = 0.f;
#pragma unroll
      for (int w = 0; w < 8; ++w) s += r2[w * 128 + tid];
      st_g(rs, I_OUT + h * DM + 128 * j + tid, s, 0.f, ta);
    }
    STAMP(4);
    if (h == 4 * j + 1) out_reduce(rs, ws, out, L, 0, 128 * j, tid, ta);
    STAMP(5);
  }
}

DI void b_role(const u16* __restrict__ xA, const u16* __restrict__ wqA, const u16* __restrict__ xB, const u16* __restrict__ wqB,
               const u16* __restrict__ ckv, const u16* __restrict__ wuk, const u16* __restrict__ wuv, const u16* __restrict__ woB,
               unsigned char* ws, u16* __restrict__ out, LAS unsigned char* L, int h, int half, int wave, int lane) {
  const int r = lane >> 4, c = lane & 15, tid = wave * 64 + lane;
  const int cl = lane & 31, hh = lane >> 5;
  const int kb_ = 2 * h + half;
  const rsrc_t rs = __builtin_amdgcn_make_buffer_rsrc((void*)ws, 0, (int)W_END, 0x00020000);
  STAMP(0);
  const Tag tb = load_tag(ws, Z_GENB);
  u32x4 cf[4][6];
  {
    u32x4 ua[12]; u32x2 ub[12];
#define LOADU() do { _Pragma("unroll") for (int i = 0; i < 12; ++i) { \
      const u16* row = wuk + ((size_t)(384 * half + 48 * wave + 4 * i + r) * NH + h) * DH; \
      ua[i] = ntload<u32x4>(row + 8 * c); ub[i] = ntload<u32x2>(row + 128 + 4 * c); } } while (0)
#define LOADC() do { _Pragma("unroll") for (int t = 0; t < 4; ++t) _Pragma("unroll") for (int i = 0; i < 6; ++i) \
        cf[t][i] = ntload<u32x4>(ckv + (size_t)(128 * kb_ + 32 * t) * DC + (size_t)(384 * wave + 64 * i + lane) * 8); } while (0)
    LOADU();
    __builtin_amdgcn_sched_barrier(0);
    if (wave >= 3) LOADC();
    __builtin_amdgcn_sched_barrier(0);
    q_gather(rs, L, h, 1, wave, lane, tb);
    STAMP(15);
    __builtin_amdgcn_sched_barrier(0);
    if (wave == 2) LOADC();
    __builtin_amdgcn_sched_barrier(0);
    WGBAR();
    STAMP(1);
    const u32x4 qa = *(const LAS u32x4*)(L + L_QS + 16 * c);
    const u32x2 qb = *(const LAS u32x2*)(L + L_QS + 256 + 8 * c);
    LAS float* qv = (LAS float*)(L + L_VEC);
#pragma unroll
    for (int i = 0; i < 12; ++i) {
      float s = 0.f;
#pragma unroll
      for (int k = 0; k < 4; ++k) s = dot2(ua[i][k], qa[k], s);
      s = dot2(ub[i][0], qb[0], s); s = dot2(ub[i][1], qb[1], s);
      s = row_sum16(s);
      if (c == 0) qv[48 * wave + 4 * i + r] = s;
    }
    WGBAR();
    if (wave < 2) {
      if (lane < 48) {
        u32x4 o;
#pragma unroll
        for (int k = 0; k < 4; ++k) {
          const float a = qv[8 * lane + 2 * k], b = qv[8 * lane + 2 * k + 1];
          const float ah = rbf(a), bh = rbf(b);
          o[k] = wave ? pack2(a - ah, b - bh) : pack2(ah, bh);
        }
        const int c8 = 48 * half + lane;
        __builtin_amdgcn_raw_buffer_store_b128(o, rs, (int)(wave ? W_QL : W_QH) + ((c8 >> 1) * 64 + h + 32 * (c8 & 1)) * 16, 0, 16);
      }
      VM0();
      if (lane == 0) st_flag(rs, (int)Z_FQABS + (2 * kb_ + wave) * 8, tb);
      __builtin_amdgcn_sched_barrier(0);
      LOADC();
    }
#undef LOADC
#undef LOADU
  }
  STAMP(2);
#pragma unroll
  for (int rd = 0; rd < 2; ++rd) {
#pragma unroll
    for (int tt = 0; tt < 2; ++tt)
#pragma unroll
      for (int i = 0; i < 6; ++i) {
        const int e = 384 * wave + 64 * i + lane;
        *(LAS u32x4*)(L + L_BIG + tt * 49664 + (e / 96) * 1552 + (e % 96) * 16) = cf[2 * rd + tt][i];
      }
    WGBAR();
#pragma unroll
    for (int tt = 0; tt < 2; ++tt)
#pragma unroll
      for (int s = 0; s < 6; ++s)
        cf[2 * rd + tt][s] = *(const LAS u32x4*)(L + L_BIG + tt * 49664 + cl * 1552 + (96 * wave + 16 * s + 8 * hh) * 2);
    WGBAR();
  }
  {
    for (unsigned spins = 0;;) {
      const u32x4 f = __builtin_amdgcn_raw_buffer_load_b128(rs, (int)Z_FQABS + lane * 16, 0, 16);
      if (__all((f[0] == tb.lo) & (f[1] == tb.hi) & (f[2] == tb.lo) & (f[3] == tb.hi))) break;
      if (spin_fail(spins)) break;
    }
    CBAR();
    STAMP(3);
    u32x4 qh[6], ql[6];
#pragma unroll
    for (int s = 0; s < 6; ++s) {
      qh[s] = __builtin_amdgcn_raw_buffer_load_b128(rs, (int)W_QH + ((wave * 6 + s) * 64 + lane) * 16, 0, 16);
      ql[s] = __builtin_amdgcn_raw_buffer_load_b128(rs, (int)W_QL + ((wave * 6 + s) * 64 + lane) * 16, 0, 16);
    }
    __builtin_amdgcn_sched_barrier(0);
    f32x16 zero;
#pragma unroll
    for (int i = 0; i < 16; ++i) zero[i] = 0.f;
    LAS f32x4* sp = (LAS f32x4*)(L + L_BIG);
#pragma unroll
    for (int t = 0; t < 4; ++t) {
      f32x16 S = zero;
#pragma unroll
      for (int s = 0; s < 6; ++s) S = MFMA32(cf[t][s], ql[s], S);
#pragma unroll
      for (int s = 0; s < 6; ++s) S = MFMA32(cf[t][s], qh[s], S);
#pragma unroll
      for (int g = 0; g < 4; ++g) sp[((wave * 4 + t) * 4 + g) * 64 + lane] = f32x4{S[4 * g], S[4 * g + 1], S[4 * g + 2], S[4 * g + 3]};
    }
    STAMP(4);
    WGBAR();
    float sv[8];
    {
      const int t = wave >> 1;
#pragma unroll
      for (int gg = 0; gg < 2; ++gg) {
        const int g = 2 * (wave & 1) + gg;
        f32x4 a = sp[((0 * 4 + t) * 4 + g) * 64 + lane];
#pragma unroll
        for (int w = 1; w < 8; ++w) a += sp[((w * 4 + t) * 4 + g) * 64 + lane];
#pragma unroll
        for (int k = 0; k < 4; ++k) sv[4 * gg + k] = rbf(rbf(a[k]) * SCALE);
      }
    }
    float m = fmaxf(fmaxf(fmaxf(sv[0], sv[1]), fmaxf(sv[2], sv[3])), fmaxf(fmaxf(sv[4], sv[5]), fmaxf(sv[6], sv[7])));
    m = fmaxf(m, __shfl_xor(m, 32));
    LAS float* mx = (LAS float*)(L + L_RED);
    if (lane < 32) mx[wave * 32 + lane] = m;
    WGBAR();
#pragma unroll
    for (int w = 0; w < 8; ++w) m = fmaxf(m, mx[w * 32 + cl]);
    float l = 0.f;
#pragma unroll
    for (int k = 0; k < 8; ++k) { sv[k] = ex2((sv[k] - m) * LOG2E); l += sv[k]; }
    l += __shfl_xor(l, 32);
    if (lane < 32) mx[256 + wave * 32 + lane] = l;
    LAS u32x4* pf = (LAS u32x4*)(L + L_SF);
    pf[wave * 64 + lane] = u32x4{pack2(sv[0], sv[1]), pack2(sv[2], sv[3]), pack2(sv[4], sv[5]), pack2(sv[6], sv[7])};
    WGBAR();
    u32x4 pb[4][2];
#pragma unroll
    for (int t = 0; t < 4; ++t) { pb[t][0] = pf[(2 * t) * 64 + lane]; pb[t][1] = pf[(2 * t + 1) * 64 + lane]; }
    if (wave == 0 && lane < 32) {
      float ls = 0.f;
#pragma unroll
      for (int w = 0; w < 8; ++w) ls += mx[256 + w * 32 + lane];
      const u32x2 ml = {asu(m), asu(ls)};
      __builtin_amdgcn_raw_buffer_store_b64(ml, rs, (int)W_BML + ((cl * NSB + kb_) * 2) * 4, 0, 16);
    }
    u32x4 idlo, idhi;
#pragma unroll
    for (int k = 0; k < 4; ++k) {
      const int k0 = 8 * hh + 2 * k, k1 = k0 + 1;
      idlo[k] = (cl == k0 ? 0x3f80u : 0u) | (cl == k1 ? 0x3f800000u : 0u);
      idhi[k] = (cl == k0 + 16 ? 0x3f80u : 0u) | (cl == k1 + 16 ? 0x3f800000u : 0u);
    }
    STAMP(5);
    LAS float* ost = (LAS float*)(L + L_BIG) + wave * 3200;
#pragma unroll
    for (int ct = 0; ct < 3; ++ct) {
      f32x16 O = zero;
#pragma unroll
      for (int t = 0; t < 4; ++t) {
        f32x16 T = MFMA32(cf[t][2 * ct], idlo, zero);
        T = MFMA32(cf[t][2 * ct + 1], idhi, T);
        O = MFMA32(pack8(T, 0), pb[t][0], O);
        O = MFMA32(pack8(T, 1), pb[t][1], O);
      }
#pragma unroll
      for (int g = 0; g < 4; ++g)
        *(LAS f32x4*)(ost + cl * 100 + 32 * ct + 8 * g + 4 * hh) = f32x4{O[4 * g], O[4 * g + 1], O[4 * g + 2], O[4 * g + 3]};
    }
    asm volatile("s_waitcnt lgkmcnt(0)" ::: "memory");
#pragma unroll
    for (int i = 0; i < 6; ++i) {
      const int e = i * 64 + lane, hd = e / 12, q8 = e % 12;
      const f32x4 v0 = *(const LAS f32x4*)(ost + hd * 100 + 8 * q8), v1 = *(const LAS f32x4*)(ost + hd * 100 + 8 * q8 + 4);
      const u32x4 o = {pack2(v0[0], v0[1]), pack2(v0[2], v0[3]), pack2(v1[0], v1[1]), pack2(v1[2], v1[3])};
      __builtin_amdgcn_raw_buffer_store_b128(o, rs, (int)W_BO + ((kb_ * NH + hd) * DC + 96 * wave + 8 * q8) * 2, 0, 16);
    }
    STAMP(6);
    VM0();
    WGBAR();
    if (tid == 0) st_flag(rs, (int)Z_FBATT + kb_ * 8, tb);
    STAMP(7);
  }
  {
    const int lane2 = opaque(lane);
    const int r = lane2 >> 4, c = lane2 & 15, tid = wave * 64 + lane2;
    if (wave != 0) {
      __builtin_amdgcn_sched_barrier(0);
    }
    u32x4 va[12]; u32x2 vb[12];
#pragma unroll
    for (int i = 0; i < 12; ++i) {
      const u16* row = wuv + ((size_t)(384 * half + 48 * wave + 4 * i + r) * NH + h) * DH;
      va[i] = ntload<u32x4>(row + 8 * c); vb[i] = ntload<u32x2>(row + 128 + 4 * c);
    }
    __builtin_amdgcn_sched_barrier(0);
    for (unsigned spins = 0;;) {
      const u32x2 f = __builtin_amdgcn_raw_buffer_load_b64(rs, (int)Z_FBATT + lane2 * 8, 0, 16);
      if (__all((f[0] == tb.lo) & (f[1] == tb.hi))) break;
      if (spin_fail(spins)) break;
    }
    CBAR();
    STAMP(8);
    const u32x2 st = __builtin_amdgcn_raw_buffer_load_b64(rs, (int)W_BML + (h * NSB + lane2) * 8, 0, 16);
    u32x4 pa[8];
    const int l48 = lane2 < 48 ? lane2 : 0;
#pragma unroll
    for (int i = 0; i < 8; ++i)
      pa[i] = __builtin_amdgcn_raw_buffer_load_b128(rs, (int)W_BO + (((8 * wave + i) * NH + h) * DC + 384 * half + 8 * l48) * 2, 0, 16);
    const float m0 = asf(st[0]), l0 = asf(st[1]);
    float M = m0;
#pragma unroll
    for (int o = 1; o < 64; o <<= 1) M = fmaxf(M, __shfl_xor(M, o));
    const float f0 = ex2((m0 - M) * LOG2E);
    float Ls = f0 * l0;
#pragma unroll
    for (int o = 1; o < 64; o <<= 1) Ls += __shfl_xor(Ls, o);
    const float w0 = f0 / Ls;
    float sa[8];
#pragma unroll
    for (int k = 0; k < 8; ++k) sa[k] = 0.f;
#pragma unroll
    for (int i = 0; i < 8; ++i) {
      const float w = __shfl(w0, 8 * wave + i);
#pragma unroll
      for (int k = 0; k < 4; ++k) { sa[2 * k] = fmaf(w, bflo(pa[i][k]), sa[2 * k]); sa[2 * k + 1] = fmaf(w, bfhi(pa[i][k]), sa[2 * k + 1]); }
    }
    LAS f32x4* acc4 = (LAS f32x4*)(L + L_RED);
    if (lane2 < 48) {
      acc4[wave * 96 + 2 * lane2] = f32x4{sa[0], sa[1], sa[2], sa[3]};
      acc4[wave * 96 + 2 * lane2 + 1] = f32x4{sa[4], sa[5], sa[6], sa[7]};
    }
    __builtin_amdgcn_sched_barrier(0);
    u32x4 wo[24];
    const int lc = lane2 < 40 ? lane2 : 0;
#pragma unroll
    for (int i = 0; i < 24; ++i) wo[i] = ntload<u32x4>(woB + (size_t)(h * DH + 24 * wave + i) * DM + 320 * half + 8 * lc);
    __builtin_amdgcn_sched_barrier(0);
    WGBAR();
    if (tid < 384) {
      const LAS float* af = (const LAS float*)(L + L_RED) + tid;
      float s = 0.f;
#pragma unroll
      for (int w = 0; w < 8; ++w) s += af[w * 384];
      ((LAS float*)(L + L_VEC))[tid] = s;
    }
    WGBAR();
    STAMP(9);
    float acc[12];
#pragma unroll
    for (int k = 0; k < 12; ++k) acc[k] = 0.f;
    const LAS float* cv = (const LAS float*)(L + L_VEC);
#pragma unroll
    for (int i = 0; i < 12; ++i) fma_row(acc, cv[48 * wave + 4 * i + r], va[i], vb[i]);
    LAS float* red = (LAS float*)(L + L_RED);
    rows_to_lds(acc, red + wave * DH, lane2);
    WGBAR();
    if (tid < DH) {
      float s = 0.f;
#pragma unroll
      for (int w = 0; w < 8; ++w) s += red[w * DH + tid];
      st_g(rs, I_CTX + (h * 2 + half) * DH + tid, s, 0.f, tb);
      float v[1];
      gather<1>(rs, I_CTX + (h * 2 + (half ^ 1)) * DH + tid, 0, tb, v);
      ((LAS float*)(L + L_VEC))[tid] = rbf(half ? v[0] + s : s + v[0]);
    }
    WGBAR();
    STAMP(10);
    float a8[8];
#pragma unroll
    for (int k = 0; k < 8; ++k) a8[k] = 0.f;
#pragma unroll
    for (int i = 0; i < 24; ++i) {
      const float x = cv[24 * wave + i];
#pragma unroll
      for (int k = 0; k < 4; ++k) { a8[2 * k] = fmaf(x, bflo(wo[i][k]), a8[2 * k]); a8[2 * k + 1] = fmaf(x, bfhi(wo[i][k]), a8[2 * k + 1]); }
    }
    LAS float* r2 = (LAS float*)(L + L_BIG);
    if (lane2 < 40) {
#pragma unroll
      for (int k = 0; k < 8; ++k) r2[wave * 320 + 8 * lane2 + k] = a8[k];
    }
    WGBAR();
    if (tid < 320) {
      float s = 0.f;
#pragma unroll
      for (int w = 0; w < 8; ++w) s += r2[w * 320 + tid];
      st_g(rs, I_OUT + (NH + h) * DM + 320 * half + tid, s, 0.f, tb);
    }
    STAMP(11);
    if ((kb_ % 12) == 3 && kb_ < 60) out_reduce(rs, ws, out, L, 1, 128 * (kb_ / 12), tid, tb);
    STAMP(12);
  }
}

__global__ void __launch_bounds__(512, 2) mega(const u16* __restrict__ xA, const u16* __restrict__ K, const u16* __restrict__ V,
                                               const u16* __restrict__ wqA, const u16* __restrict__ woA,
                                               const u16* __restrict__ xB, const u16* __restrict__ ckv, const u16* __restrict__ wqB,
                                               const u16* __restrict__ wuk, const u16* __restrict__ wuv, const u16* __restrict__ woB,
                                               unsigned char* ws, u16* __restrict__ out) {
  extern __shared__ __attribute__((aligned(16))) unsigned char smem[];
  LAS unsigned char* L = (LAS unsigned char*)smem;
  const int tid = threadIdx.x, lane = tid & 63;
  const int wave = __builtin_amdgcn_readfirstlane(tid >> 6);
  const int b = blockIdx.x;
  const int h = (b & 7) * 4 + (b >> 6), jj = (b >> 3) & 7;
  if (jj < NA) a_role(xA, wqA, xB, wqB, K, V, woA, ws, out, L, h, jj, wave, lane);
  else b_role(xA, wqA, xB, wqB, ckv, wuk, wuv, woB, ws, out, L, h, jj - NA, wave, lane);
}

extern "C" void kernel_launch(void* const* d_in, const int* in_sizes, int n_in,
                              void* d_out, int out_size, void* d_ws, size_t ws_size,
                              hipStream_t stream) {
  (void)hipFuncSetAttribute((const void*)mega, hipFuncAttributeMaxDynamicSharedMemorySize, L_BYTES);
  mega<<<256, 512, L_BYTES, stream>>>((const u16*)d_in[0], (const u16*)d_in[1], (const u16*)d_in[2], (const u16*)d_in[3], (const u16*)d_in[4],
                                      (const u16*)d_in[5], (const u16*)d_in[6], (const u16*)d_in[7], (const u16*)d_in[8], (const u16*)d_in[9],
                                      (const u16*)d_in[10], (unsigned char*)d_ws, (u16*)d_out);
}
```

```cpp
#include <hip/hip_runtime.h>
#include <hip/hip_bf16.h>
#include <stdint.h>

typedef unsigned u32x4 __attribute__((ext_vector_type(4)));
typedef unsigned u32x2 __attribute__((ext_vector_type(2)));
typedef __bf16 bf16x2 __attribute__((ext_vector_type(2)));
typedef __bf16 bf16x8 __attribute__((ext_vector_type(8)));
typedef float f32x16 __attribute__((ext_vector_type(16)));
typedef float f32x4 __attribute__((ext_vector_type(4)));
typedef unsigned short u16;
typedef unsigned long long u64;

#define DI __device__ __forceinline__
#define GAS __attribute__((address_space(1)))
#define LAS __attribute__((address_space(3)))
#define RLX_AGENT __ATOMIC_RELAXED, __HIP_MEMORY_SCOPE_AGENT

constexpr int NH = 32, SK = 8192, DH = 192, DM = 640, DC = 768, HD = NH * DH;
constexpr float SCALE = 0.07216878364870322f;
constexpr float LOG2E = 1.4426950408889634f;
#define A_KR 2
#define A_NB 1
#define EARLY_W 3
#define POLL_SLEEP 4
constexpr int NA = 6;
constexpr int NSB = 64;
constexpr unsigned kSpinCap = 1u << 18;

constexpr size_t Z_GENA = 0, Z_GENB = 128;
constexpr size_t Z_FQABS = 1024;
constexpr size_t Z_FBATT = 2048;
constexpr size_t Z_GRAN = 4096;
constexpr int I_QP = 0;
constexpr int I_CTX = I_QP + NH * 5 * 384;
constexpr int I_AP = I_CTX + NH * 2 * 192;
constexpr int I_OUT = I_AP + NH * NA * 200;
constexpr int I_END = I_OUT + 2 * NH * DM;
constexpr size_t W_QH = Z_GRAN + (size_t)I_END * 16;
constexpr size_t W_QL = W_QH + (size_t)NH * DC * 2;
constexpr size_t W_BML = W_QL + (size_t)NH * DC * 2;
constexpr size_t W_BO = W_BML + (size_t)NH * NSB * 2 * 4;
constexpr size_t W_END = W_BO + (size_t)NSB * NH * DC * 2;
static_assert(W_END < (1ull << 31), "ws layout");

constexpr int L_QS = 0;
constexpr int L_VEC = 512;
constexpr int L_RED = 2048;
constexpr int L_BIG = 16384;
constexpr int L_SF = L_BIG + 131072;
constexpr int L_BYTES = L_SF + 16384;

DI float dot2(unsigned a, unsigned b, float c) {
  return __builtin_amdgcn_fdot2_f32_bf16(__builtin_bit_cast(bf16x2, a), __builtin_bit_cast(bf16x2, b), c, false);
}
DI float rbf(float x) { return (float)(__bf16)x; }
DI float asf(unsigned v) { return __builtin_bit_cast(float, v); }
DI unsigned asu(float v) { return __builtin_bit_cast(unsigned, v); }
DI float bflo(unsigned v) { return __builtin_bit_cast(float, v << 16); }
DI float bfhi(unsigned v) { return __builtin_bit_cast(float, v & 0xffff0000u); }
DI u16 f2bf(float x) { return __builtin_bit_cast(u16, (__bf16)x); }
DI unsigned pack2(float lo, float hi) { return (unsigned)f2bf(lo) | ((unsigned)f2bf(hi) << 16); }
template <int CTRL> DI float dpp(float x) {
  return __builtin_bit_cast(float, __builtin_amdgcn_mov_dpp(__builtin_bit_cast(int, x), CTRL, 0xf, 0xf, true));
}
DI float row_sum16(float x) {
  x += dpp<0x128>(x); x += dpp<0x124>(x); x += dpp<0x122>(x); x += dpp<0x121>(x); return x;
}
DI float ex2(float x) { return __builtin_amdgcn_exp2f(x); }
template <typename T> DI T ntload(const void* p) { return __builtin_nontemporal_load(reinterpret_cast<const T*>(p)); }
#define MFMA32(a, b, c) __builtin_amdgcn_mfma_f32_32x32x16_bf16(__builtin_bit_cast(bf16x8, a), __builtin_bit_cast(bf16x8, b), (c), 0, 0, 0)
DI u32x4 pack8(const f32x16& x, int s) {
  u32x4 p;
#pragma unroll
  for (int j = 0; j < 4; ++j) p[j] = pack2(x[8 * s + 2 * j], x[8 * s + 2 * j + 1]);
  return p;
}
DI int opaque(int x) { asm volatile("" : "+v"(x)); return x; }
#define VM0() asm volatile("s_waitcnt vmcnt(0)" ::: "memory")
#define CBAR() asm volatile("" ::: "memory")
#define WGBAR() do { asm volatile("s_waitcnt lgkmcnt(0)" ::: "memory"); __builtin_amdgcn_s_barrier(); asm volatile("" ::: "memory"); } while (0)

#define STAMP(k) do { } while (0)

typedef __amdgpu_buffer_rsrc_t rsrc_t;
struct Tag { unsigned lo, hi; };
DI Tag load_tag(unsigned char* ws, size_t off) {
  const u64 g = __hip_atomic_load((GAS u64*)(ws + off), RLX_AGENT) + 1ull;
  Tag t;
  t.lo = __builtin_amdgcn_readfirstlane((unsigned)g);
  t.hi = __builtin_amdgcn_readfirstlane((unsigned)(g >> 32));
  return t;
}
DI void st_g(rsrc_t rs, int idx, float v0, float v1, Tag t) {
  const u32x4 x = {asu(v0), asu(v1), t.lo, t.hi};
  __builtin_amdgcn_raw_buffer_store_b128(x, rs, (int)Z_GRAN + idx * 16, 0, 16);
}
DI u32x4 ld_g(rsrc_t rs, int idx) { return __builtin_amdgcn_raw_buffer_load_b128(rs, (int)Z_GRAN + idx * 16, 0, 16); }
DI bool tag_ok(const u32x4& x, Tag t) { return (x[2] == t.lo) & (x[3] == t.hi); }
DI void st_flag(rsrc_t rs, int off, Tag t) {
  const u32x2 x = {t.lo, t.hi};
  __builtin_amdgcn_raw_buffer_store_b64(x, rs, off, 0, 16);
}
DI bool spin_fail(unsigned& spins) {
  __builtin_amdgcn_s_sleep(1);
  return ++spins > kSpinCap;
}
DI void prepoll(rsrc_t rs, int idx, int stride, int n, Tag t, int lane) {
  for (unsigned spins = 0;;) {
    bool ok = true;
    if (lane < n) ok = tag_ok(ld_g(rs, idx + lane * stride), t);
    if (__all(ok)) break;
    __builtin_amdgcn_s_sleep(POLL_SLEEP);
    if (++spins > kSpinCap) break;
  }
  CBAR();
}
template <int N> DI void gather(rsrc_t rs, int idx, int stride, Tag t, float (&v)[N]) {
  for (unsigned spins = 0;;) {
    bool ok = true;
#pragma unroll
    for (int k = 0; k < N; ++k) {
      const u32x4 x = ld_g(rs, idx + k * stride);
      v[k] = asf(x[0]);
      ok &= tag_ok(x, t);
    }
    if (__all(ok)) break;
    if (spin_fail(spins)) break;
  }
  CBAR();
}

DI void fma_row(float (&acc)[12], float p, const u32x4& a, const u32x2& b) {
#pragma unroll
  for (int k = 0; k < 4; ++k) { acc[2 * k] = fmaf(p, bflo(a[k]), acc[2 * k]); acc[2 * k + 1] = fmaf(p, bfhi(a[k]), acc[2 * k + 1]); }
#pragma unroll
  for (int k = 0; k < 2; ++k) { acc[8 + 2 * k] = fmaf(p, bflo(b[k]), acc[8 + 2 * k]); acc[8 + 2 * k + 1] = fmaf(p, bfhi(b[k]), acc[8 + 2 * k + 1]); }
}
DI void rows_to_lds(float (&acc)[12], LAS float* dst, int lane) {
  const int r = lane >> 4, c = lane & 15;
#pragma unroll
  for (int k = 0; k < 12; ++k) { float a = acc[k]; a += __shfl_xor(a, 16); a += __shfl_xor(a, 32); acc[k] = a; }
  if (r == 0) {
#pragma unroll
    for (int k = 0; k < 8; ++k) dst[8 * c + k] = acc[k];
#pragma unroll
    for (int k = 0; k < 4; ++k) dst[128 + 4 * c + k] = acc[8 + k];
  }
}

DI void out_reduce(rsrc_t rs, unsigned char* ws, u16* __restrict__ out, LAS unsigned char* L, int mat, int col0, int tid, Tag t) {
  const int col = tid & 127, qd = tid >> 7;
  float v[8];
  prepoll(rs, I_OUT + (mat * NH + qd * 8) * DM + col0 + (col | 63), DM, 8, t, tid & 63);
  gather<8>(rs, I_OUT + (mat * NH + qd * 8) * DM + col0 + col, DM, t, v);
  LAS float* red = (LAS float*)(L + L_RED);
  red[tid] = ((v[0] + v[1]) + (v[2] + v[3])) + ((v[4] + v[5]) + (v[6] + v[7]));
  WGBAR();
  if (tid < 128) out[mat * DM + col0 + tid] = f2bf((red[tid] + red[128 + tid]) + (red[256 + tid] + red[384 + tid]));
  if (col0 == 0 && tid == 0) st_flag(rs, (int)(mat ? Z_GENB : Z_GENA), t);
}

constexpr int NQ = 5;
struct QLoads { u32x4 a[8]; u32x2 b[8]; float x[8]; };
DI void q_issue(QLoads& Q, const u16* __restrict__ xA, const u16* __restrict__ wqA, const u16* __restrict__ xB,
                const u16* __restrict__ wqB, int h, int jj, int wave, int lane) {
  const int r = lane >> 4, c = lane & 15;
  const u16* x = wave < 4 ? xA : xB;
  const u16* w = wave < 4 ? wqA : wqB;
#pragma unroll
  for (int i = 0; i < 8; ++i) {
    const int row = 128 * jj + 32 * (wave & 3) + 4 * i + r;
    const u16* p = w + (size_t)row * HD + h * DH;
    Q.a[i] = ntload<u32x4>(p + 8 * c); Q.b[i] = ntload<u32x2>(p + 128 + 4 * c);
    Q.x[i] = bflo((unsigned)x[row]);
  }
}
DI void q_partial(QLoads& Q, LAS unsigned char* L, int wave, int lane) {
  float acc[12];
#pragma unroll
  for (int k = 0; k < 12; ++k) acc[k] = 0.f;
#pragma unroll
  for (int i = 0; i < 8; ++i) fma_row(acc, Q.x[i], Q.a[i], Q.b[i]);
  rows_to_lds(acc, (LAS float*)(L + L_RED) + wave * DH, lane);
}
DI void q_publish(rsrc_t rs, LAS unsigned char* L, int h, int jj, int wave, int lane, Tag ta, Tag tb) {
  const int tid = wave * 64 + lane;
  const LAS float* red = (const LAS float*)(L + L_RED);
  if (wave < 6) {
    const int mt = wave >= 3 ? 1 : 0, d = tid - mt * DH;
    const LAS float* rp = red + mt * 4 * DH + d;
    st_g(rs, I_QP + (h * NQ + jj) * 384 + tid, (rp[0] + rp[DH]) + (rp[2 * DH] + rp[3 * DH]), 0.f, mt ? tb : ta);
  }
}
DI void q_gather(rsrc_t rs, LAS unsigned char* L, int h, int part, int wave, int lane, Tag t) {
  const int tid = wave * 64 + lane;
  if (wave < 3) {
    float v[NQ];
    prepoll(rs, I_QP + h * NQ * 384 + part * DH + wave * 64 + 63, 384, NQ, t, lane);
    gather<NQ>(rs, I_QP + h * NQ * 384 + part * DH + tid, 384, t, v);
    ((LAS u16*)(L + L_QS))[tid] = f2bf(((v[0] + v[1]) + (v[2] + v[3])) + v[4]);
  }
}

DI void a_role(const u16* __restrict__ xA, const u16* __restrict__ wqA, const u16* __restrict__ xB, const u16* __restrict__ wqB,
               const u16* __restrict__ K, const u16* __restrict__ V, const u16* __restrict__ woA,
               unsigned char* ws, u16* __restrict__ out, LAS unsigned char* L, int h, int j, int wave, int lane) {
  const int r = lane >> 4, c = lane & 15, tid = wave * 64 + lane;
  STAMP(0);
  const rsrc_t rs = __builtin_amdgcn_make_buffer_rsrc((void*)ws, 0, (int)W_END, 0x00020000);
  const Tag ta = load_tag(ws, Z_GENA), tb = load_tag(ws, Z_GENB);
  QLoads Q;
  if (j < NQ) q_issue(Q, xA, wqA, xB, wqB, h, j, wave, lane);
  constexpr int KR = A_KR, NB = A_NB, TK = 4 * KR, NTH = SK / TK;
  const int gw = j * 8 + wave;
  const int t_beg = (NTH * gw) / 48, t_end = (NTH * (gw + 1)) / 48, NT = t_end - t_beg;
  const size_t base = ((size_t)h * SK + t_beg * TK + r) * DH;
  const u16* kp = K + base;
  const u16* vp = V + base;
  u32x4 ka[NB][KR], va[NB][KR]; u32x2 kb[NB][KR], vb[NB][KR];
#define LOADT(buf, t) do { _Pragma("unroll") for (int i = 0; i < KR; ++i) { \
    const u16* kr = kp + (size_t)((t) * TK + i * 4) * DH; const u16* vr = vp + (size_t)((t) * TK + i * 4) * DH; \
    ka[buf][i] = ntload<u32x4>(kr + 8 * c); kb[buf][i] = ntload<u32x2>(kr + 128 + 4 * c); \
    va[buf][i] = ntload<u32x4>(vr + 8 * c); vb[buf][i] = ntload<u32x2>(vr + 128 + 4 * c); } } while (0)
  if (j < NQ) q_partial(Q, L, wave, lane);
  STAMP(10);
  WGBAR();
  STAMP(11);
  __builtin_amdgcn_sched_barrier(0);
  if (wave >= EARLY_W) {
#pragma unroll
    for (int b = 0; b < NB; ++b) LOADT(b, b);
  }
  __builtin_amdgcn_sched_barrier(0);
  if (j < NQ) q_publish(rs, L, h, j, wave, lane, ta, tb);
  q_gather(rs, L, h, 0, wave, lane, ta);
  STAMP(12);
  __builtin_amdgcn_sched_barrier(0);
  if (wave < EARLY_W) {
#pragma unroll
    for (int b = 0; b < NB; ++b) LOADT(b, b);
  }
  __builtin_amdgcn_sched_barrier(0);
  WGBAR();
  STAMP(1);
  const u32x4 qa = *(const LAS u32x4*)(L + L_QS + 16 * c);
  const u32x2 qb = *(const LAS u32x2*)(L + L_QS + 256 + 8 * c);
  float m = -INFINITY, l = 0.f, acc[12];
#pragma unroll
  for (int k = 0; k < 12; ++k) acc[k] = 0.f;
  for (int t0 = 0; t0 < NT; t0 += NB) {
#pragma unroll
    for (int b = 0; b < NB; ++b) {
      const int t = t0 + b;
      if (t < NT) {
        float s[KR];
        float mn = m;
#pragma unroll
        for (int i = 0; i < KR; ++i) {
          float d = 0.f;
#pragma unroll
          for (int k = 0; k < 4; ++k) d = dot2(ka[b][i][k], qa[k], d);
          d = dot2(kb[b][i][0], qb[0], d); d = dot2(kb[b][i][1], qb[1], d);
          d = row_sum16(d);
          s[i] = rbf(rbf(d) * SCALE);
          mn = fmaxf(mn, s[i]);
        }
        const float alpha = ex2((m - mn) * LOG2E);
        m = mn;
        float ps = 0.f;
#pragma unroll
        for (int i = 0; i < KR; ++i) { s[i] = ex2((s[i] - mn) * LOG2E); ps += s[i]; }
        l = l * alpha + ps;
#pragma unroll
        for (int k = 0; k < 12; ++k) acc[k] *= alpha;
#pragma unroll
        for (int i = 0; i < KR; ++i) fma_row(acc, s[i], va[b][i], vb[b][i]);
        if (t + NB < NT) LOADT(b, t + NB);
      }
    }
  }
#undef LOADT
  STAMP(2);
  u32x4 wv[6];
  if (j < 5) {
#pragma unroll
    for (int i = 0; i < 6; ++i) wv[i] = ntload<u32x4>(woA + (size_t)(h * DH + 24 * wave + 4 * i + r) * DM + 128 * j + 8 * c);
  }
  float M = fmaxf(m, __shfl_xor(m, 16)); M = fmaxf(M, __shfl_xor(M, 32));
  const float f = ex2((m - M) * LOG2E);
  l *= f; l += __shfl_xor(l, 16); l += __shfl_xor(l, 32);
#pragma unroll
  for (int k = 0; k < 12; ++k) acc[k] *= f;
  LAS float* red = (LAS float*)(L + L_RED);
  rows_to_lds(acc, red + wave * 200 + 8, lane);
  if (lane == 0) { red[wave * 200] = M; red[wave * 200 + 1] = l; }
  WGBAR();
  {
    float mw[8], MM = -INFINITY;
#pragma unroll
    for (int w = 0; w < 8; ++w) { mw[w] = red[w * 200]; MM = fmaxf(MM, mw[w]); }
    const int idx = tid < DH ? 8 + tid : 1;
    float s = 0.f;
#pragma unroll
    for (int w = 0; w < 8; ++w) s = fmaf(ex2((mw[w] - MM) * LOG2E), red[w * 200 + idx], s);
    const int ga = I_AP + (h * NA + j) * 200;
    if (tid < DH) st_g(rs, ga + 8 + tid, s, 0.f, ta);
    if (tid == DH) st_g(rs, ga, MM, s, ta);
  }
  if (j < 5) {
    if (wave < 3) {
      float v[NA], mm[NA], ll[NA];
      const int gh = I_AP + h * NA * 200;
      prepoll(rs, gh, 200, NA, ta, lane);
      for (unsigned spins = 0;;) {
        bool ok = true;
#pragma unroll
        for (int k = 0; k < NA; ++k) {
          const u32x4 x = ld_g(rs, gh + k * 200 + 8 + tid), y = ld_g(rs, gh + k * 200);
          v[k] = asf(x[0]); mm[k] = asf(y[0]); ll[k] = asf(y[1]);
          ok &= tag_ok(x, ta) & tag_ok(y, ta);
        }
        if (__all(ok)) break;
        if (spin_fail(spins)) break;
      }
      CBAR();
      float Mx = mm[0];
#pragma unroll
      for (int k = 1; k < NA; ++k) Mx = fmaxf(Mx, mm[k]);
      float Lsum = 0.f, o = 0.f;
#pragma unroll
      for (int k = 0; k < NA; ++k) { const float g = ex2((mm[k] - Mx) * LOG2E); Lsum = fmaf(g, ll[k], Lsum); o = fmaf(g, v[k], o); }
      ((LAS float*)(L + L_VEC))[tid] = rbf(o / Lsum);
    }
    WGBAR();
    STAMP(3);
    const LAS float* vec = (const LAS float*)(L + L_VEC);
    float a8[8];
#pragma unroll
    for (int k = 0; k < 8; ++k) a8[k] = 0.f;
#pragma unroll
    for (int i = 0; i < 6; ++i) {
      const float x = vec[24 * wave + 4 * i + r];
#pragma unroll
      for (int k = 0; k < 4; ++k) { a8[2 * k] = fmaf(x, bflo(wv[i][k]), a8[2 * k]); a8[2 * k + 1] = fmaf(x, bfhi(wv[i][k]), a8[2 * k + 1]); }
    }
#pragma unroll
    for (int k = 0; k < 8; ++k) { a8[k] += __shfl_xor(a8[k], 16); a8[k] += __shfl_xor(a8[k], 32); }
    LAS float* r2 = (LAS float*)(L + L_BIG);
    if (r == 0) {
#pragma unroll
      for (int k = 0; k < 8; ++k) r2[wave * 128 + 8 * c + k] = a8[k];
    }
    WGBAR();
    if (tid < 128) {
      float s = 0.f;
#pragma unroll
      for (int w = 0; w < 8; ++w) s += r2[w * 128 + tid];
      st_g(rs, I_OUT + h * DM + 128 * j + tid, s, 0.f, ta);
    }
    STAMP(4);
    if (h == 4 * j + 1) out_reduce(rs, ws, out, L, 0, 128 * j, tid, ta);
    STAMP(5);
  }
}

DI void b_role(const u16* __restrict__ xA, const u16* __restrict__ wqA, const u16* __restrict__ xB, const u16* __restrict__ wqB,
               const u16* __restrict__ ckv, const u16* __restrict__ wuk, const u16* __restrict__ wuv, const u16* __restrict__ woB,
               unsigned char* ws, u16* __restrict__ out, LAS unsigned char* L, int h, int half, int wave, int lane) {
  const int r = lane >> 4, c = lane & 15, tid = wave * 64 + lane;
  const int cl = lane & 31, hh = lane >> 5;
  const int kb_ = 2 * h + half;
  const rsrc_t rs = __builtin_amdgcn_make_buffer_rsrc((void*)ws, 0, (int)W_END, 0x00020000);
  STAMP(0);
  const Tag tb = load_tag(ws, Z_GENB);
  u32x4 cf[4][6];
  {
    u32x4 ua[12]; u32x2 ub[12];
#define LOADU() do { _Pragma("unroll") for (int i = 0; i < 12; ++i) { \
      const u16* row = wuk + ((size_t)(384 * half + 48 * wave + 4 * i + r) * NH + h) * DH; \
      ua[i] = ntload<u32x4>(row + 8 * c); ub[i] = ntload<u32x2>(row + 128 + 4 * c); } } while (0)
#define LOADC() do { _Pragma("unroll") for (int t = 0; t < 4; ++t) _Pragma("unroll") for (int i = 0; i < 6; ++i) \
        cf[t][i] = ntload<u32x4>(ckv + (size_t)(128 * kb_ + 32 * t) * DC + (size_t)(384 * wave + 64 * i + lane) * 8); } while (0)
    LOADU();
    __builtin_amdgcn_sched_barrier(0);
    if (wave >= 3) LOADC();
    __builtin_amdgcn_sched_barrier(0);
    q_gather(rs, L, h, 1, wave, lane, tb);
    STAMP(15);
    __builtin_amdgcn_sched_barrier(0);
    if (wave == 2) LOADC();
    __builtin_amdgcn_sched_barrier(0);
    WGBAR();
    STAMP(1);
    const u32x4 qa = *(const LAS u32x4*)(L + L_QS + 16 * c);
    const u32x2 qb = *(const LAS u32x2*)(L + L_QS + 256 + 8 * c);
    LAS float* qv = (LAS float*)(L + L_VEC);
#pragma unroll
    for (int i = 0; i < 12; ++i) {
      float s = 0.f;
#pragma unroll
      for (int k = 0; k < 4; ++k) s = dot2(ua[i][k], qa[k], s);
      s = dot2(ub[i][0], qb[0], s); s = dot2(ub[i][1], qb[1], s);
      s = row_sum16(s);
      if (c == 0) qv[48 * wave + 4 * i + r] = s;
    }
    WGBAR();
    if (wave < 2) {
      if (lane < 48) {
        u32x4 o;
#pragma unroll
        for (int k = 0; k < 4; ++k) {
          const float a = qv[8 * lane + 2 * k], b = qv[8 * lane + 2 * k + 1];
          const float ah = rbf(a), bh = rbf(b);
          o[k] = wave ? pack2(a - ah, b - bh) : pack2(ah, bh);
        }
        const int c8 = 48 * half + lane;
        __builtin_amdgcn_raw_buffer_store_b128(o, rs, (int)(wave ? W_QL : W_QH) + ((c8 >> 1) * 64 + h + 32 * (c8 & 1)) * 16, 0, 16);
      }
      VM0();
      if (lane == 0) st_flag(rs, (int)Z_FQABS + (2 * kb_ + wave) * 8, tb);
      __builtin_amdgcn_sched_barrier(0);
      LOADC();
    }
#undef LOADC
#undef LOADU
  }
  STAMP(2);
#pragma unroll
  for (int rd = 0; rd < 2; ++rd) {
#pragma unroll
    for (int tt = 0; tt < 2; ++tt)
#pragma unroll
      for (int i = 0; i < 6; ++i) {
        const int e = 384 * wave + 64 * i + lane;
        *(LAS u32x4*)(L + L_BIG + tt * 49664 + (e / 96) * 1552 + (e % 96) * 16) = cf[2 * rd + tt][i];
      }
    WGBAR();
#pragma unroll
    for (int tt = 0; tt < 2; ++tt)
#pragma unroll
      for (int s = 0; s < 6; ++s)
        cf[2 * rd + tt][s] = *(const LAS u32x4*)(L + L_BIG + tt * 49664 + cl * 1552 + (96 * wave + 16 * s + 8 * hh) * 2);
    WGBAR();
  }
  {
    if (wave == 0) {
      for (unsigned spins = 0;;) {
        const u32x4 f = __builtin_amdgcn_raw_buffer_load_b128(rs, (int)Z_FQABS + lane * 16, 0, 16);
        if (__all((f[0] == tb.lo) & (f[1] == tb.hi) & (f[2] == tb.lo) & (f[3] == tb.hi))) break;
        __builtin_amdgcn_s_sleep(POLL_SLEEP);
        if (++spins > kSpinCap) break;
      }
    }
    WGBAR();
    STAMP(3);
    u32x4 qh[6], ql[6];
#pragma unroll
    for (int s = 0; s < 6; ++s) {
      qh[s] = __builtin_amdgcn_raw_buffer_load_b128(rs, (int)W_QH + ((wave * 6 + s) * 64 + lane) * 16, 0, 16);
      ql[s] = __builtin_amdgcn_raw_buffer_load_b128(rs, (int)W_QL + ((wave * 6 + s) * 64 + lane) * 16, 0, 16);
    }
    __builtin_amdgcn_sched_barrier(0);
    f32x16 zero;
#pragma unroll
    for (int i = 0; i < 16; ++i) zero[i] = 0.f;
    LAS f32x4* sp = (LAS f32x4*)(L + L_BIG);
#pragma unroll
    for (int t = 0; t < 4; ++t) {
      f32x16 S = zero;
#pragma unroll
      for (int s = 0; s < 6; ++s) S = MFMA32(cf[t][s], ql[s], S);
#pragma unroll
      for (int s = 0; s < 6; ++s) S = MFMA32(cf[t][s], qh[s], S);
#pragma unroll
      for (int g = 0; g < 4; ++g) sp[((wave * 4 + t) * 4 + g) * 64 + lane] = f32x4{S[4 * g], S[4 * g + 1], S[4 * g + 2], S[4 * g + 3]};
    }
    STAMP(4);
    WGBAR();
    float sv[8];
    {
      const int t = wave >> 1;
#pragma unroll
      for (int gg = 0; gg < 2; ++gg) {
        const int g = 2 * (wave & 1) + gg;
        f32x4 a = sp[((0 * 4 + t) * 4 + g) * 64 + lane];
#pragma unroll
        for (int w = 1; w < 8; ++w) a += sp[((w * 4 + t) * 4 + g) * 64 + lane];
#pragma unroll
        for (int k = 0; k < 4; ++k) sv[4 * gg + k] = rbf(rbf(a[k]) * SCALE);
      }
    }
    float m = fmaxf(fmaxf(fmaxf(sv[0], sv[1]), fmaxf(sv[2], sv[3])), fmaxf(fmaxf(sv[4], sv[5]), fmaxf(sv[6], sv[7])));
    m = fmaxf(m, __shfl_xor(m, 32));
    LAS float* mx = (LAS float*)(L + L_RED);
    if (lane < 32) mx[wave * 32 + lane] = m;
    WGBAR();
#pragma unroll
    for (int w = 0; w < 8; ++w) m = fmaxf(m, mx[w * 32 + cl]);
    float l = 0.f;
#pragma unroll
    for (int k = 0; k < 8; ++k) { sv[k] = ex2((sv[k] - m) * LOG2E); l += sv[k]; }
    l += __shfl_xor(l, 32);
    if (lane < 32) mx[256 + wave * 32 + lane] = l;
    LAS u32x4* pf = (LAS u32x4*)(L + L_SF);
    pf[wave * 64 + lane] = u32x4{pack2(sv[0], sv[1]), pack2(sv[2], sv[3]), pack2(sv[4], sv[5]), pack2(sv[6], sv[7])};
    WGBAR();
    u32x4 pb[4][2];
#pragma unroll
    for (int t = 0; t < 4; ++t) { pb[t][0] = pf[(2 * t) * 64 + lane]; pb[t][1] = pf[(2 * t + 1) * 64 + lane]; }
    if (wave == 0 && lane < 32) {
      float ls = 0.f;
#pragma unroll
      for (int w = 0; w < 8; ++w) ls += mx[256 + w * 32 + lane];
      const u32x2 ml = {asu(m), asu(ls)};
      __builtin_amdgcn_raw_buffer_store_b64(ml, rs, (int)W_BML + ((cl * NSB + kb_) * 2) * 4, 0, 16);
    }
    u32x4 idlo, idhi;
#pragma unroll
    for (int k = 0; k < 4; ++k) {
      const int k0 = 8 * hh + 2 * k, k1 = k0 + 1;
      idlo[k] = (cl == k0 ? 0x3f80u : 0u) | (cl == k1 ? 0x3f800000u : 0u);
      idhi[k] = (cl == k0 + 16 ? 0x3f80u : 0u) | (cl == k1 + 16 ? 0x3f800000u : 0u);
    }
    STAMP(5);
    LAS float* ost = (LAS float*)(L + L_BIG) + wave * 3200;
#pragma unroll
    for (int ct = 0; ct < 3; ++ct) {
      f32x16 O = zero;
#pragma unroll
      for (int t = 0; t < 4; ++t) {
        f32x16 T = MFMA32(cf[t][2 * ct], idlo, zero);
        T = MFMA32(cf[t][2 * ct + 1], idhi, T);
        O = MFMA32(pack8(T, 0), pb[t][0], O);
        O = MFMA32(pack8(T, 1), pb[t][1], O);
      }
#pragma unroll
      for (int g = 0; g < 4; ++g)
        *(LAS f32x4*)(ost + cl * 100 + 32 * ct + 8 * g + 4 * hh) = f32x4{O[4 * g], O[4 * g + 1], O[4 * g + 2], O[4 * g + 3]};
    }
    asm volatile("s_waitcnt lgkmcnt(0)" ::: "memory");
#pragma unroll
    for (int i = 0; i < 6; ++i) {
      const int e = i * 64 + lane, hd = e / 12, q8 = e % 12;
      const f32x4 v0 = *(const LAS f32x4*)(ost + hd * 100 + 8 * q8), v1 = *(const LAS f32x4*)(ost + hd * 100 + 8 * q8 + 4);
      const u32x4 o = {pack2(v0[0], v0[1]), pack2(v0[2], v0[3]), pack2(v1[0], v1[1]), pack2(v1[2], v1[3])};
      __builtin_amdgcn_raw_buffer_store_b128(o, rs, (int)W_BO + ((kb_ * NH + hd) * DC + 96 * wave + 8 * q8) * 2, 0, 16);
    }
    STAMP(6);
    VM0();
    WGBAR();
    if (tid == 0) st_flag(rs, (int)Z_FBATT + kb_ * 8, tb);
    STAMP(7);
  }
  {
    const int lane2 = opaque(lane);
    const int r = lane2 >> 4, c = lane2 & 15, tid = wave * 64 + lane2;
    if (wave != 0) {
      __builtin_amdgcn_sched_barrier(0);
    }
    u32x4 va[12]; u32x2 vb[12];
#pragma unroll
    for (int i = 0; i < 12; ++i) {
      const u16* row = wuv + ((size_t)(384 * half + 48 * wave + 4 * i + r) * NH + h) * DH;
      va[i] = ntload<u32x4>(row + 8 * c); vb[i] = ntload<u32x2>(row + 128 + 4 * c);
    }
    __builtin_amdgcn_sched_barrier(0);
    if (wave == 0) {
      for (unsigned spins = 0;;) {
        const u32x2 f = __builtin_amdgcn_raw_buffer_load_b64(rs, (int)Z_FBATT + lane2 * 8, 0, 16);
        if (__all((f[0] == tb.lo) & (f[1] == tb.hi))) break;
        __builtin_amdgcn_s_sleep(POLL_SLEEP);
        if (++spins > kSpinCap) break;
      }
    }
    WGBAR();
    STAMP(8);
    const u32x2 st = __builtin_amdgcn_raw_buffer_load_b64(rs, (int)W_BML + (h * NSB + lane2) * 8, 0, 16);
    u32x4 pa[8];
    const int l48 = lane2 < 48 ? lane2 : 0;
#pragma unroll
    for (int i = 0; i < 8; ++i)
      pa[i] = __builtin_amdgcn_raw_buffer_load_b128(rs, (int)W_BO + (((8 * wave + i) * NH + h) * DC + 384 * half + 8 * l48) * 2, 0, 16);
    const float m0 = asf(st[0]), l0 = asf(st[1]);
    float M = m0;
#pragma unroll
    for (int o = 1; o < 64; o <<= 1) M = fmaxf(M, __shfl_xor(M, o));
    const float f0 = ex2((m0 - M) * LOG2E);
    float Ls = f0 * l0;
#pragma unroll
    for (int o = 1; o < 64; o <<= 1) Ls += __shfl_xor(Ls, o);
    const float w0 = f0 / Ls;
    float sa[8];
#pragma unroll
    for (int k = 0; k < 8; ++k) sa[k] = 0.f;
#pragma unroll
    for (int i = 0; i < 8; ++i) {
      const float w = __shfl(w0, 8 * wave + i);
#pragma unroll
      for (int k = 0; k < 4; ++k) { sa[2 * k] = fmaf(w, bflo(pa[i][k]), sa[2 * k]); sa[2 * k + 1] = fmaf(w, bfhi(pa[i][k]), sa[2 * k + 1]); }
    }
    LAS f32x4* acc4 = (LAS f32x4*)(L + L_RED);
    if (lane2 < 48) {
      acc4[wave * 96 + 2 * lane2] = f32x4{sa[0], sa[1], sa[2], sa[3]};
      acc4[wave * 96 + 2 * lane2 + 1] = f32x4{sa[4], sa[5], sa[6], sa[7]};
    }
    __builtin_amdgcn_sched_barrier(0);
    u32x4 wo[24];
    const int lc = lane2 < 40 ? lane2 : 0;
#pragma unroll
    for (int i = 0; i < 24; ++i) wo[i] = ntload<u32x4>(woB + (size_t)(h * DH + 24 * wave + i) * DM + 320 * half + 8 * lc);
    __builtin_amdgcn_sched_barrier(0);
    WGBAR();
    if (tid < 384) {
      const LAS float* af = (const LAS float*)(L + L_RED) + tid;
      float s = 0.f;
#pragma unroll
      for (int w = 0; w < 8; ++w) s += af[w * 384];
      ((LAS float*)(L + L_VEC))[tid] = s;
    }
    WGBAR();
    STAMP(9);
    float acc[12];
#pragma unroll
    for (int k = 0; k < 12; ++k) acc[k] = 0.f;
    const LAS float* cv = (const LAS float*)(L + L_VEC);
#pragma unroll
    for (int i = 0; i < 12; ++i) fma_row(acc, cv[48 * wave + 4 * i + r], va[i], vb[i]);
    LAS float* red = (LAS float*)(L + L_RED);
    rows_to_lds(acc, red + wave * DH, lane2);
    WGBAR();
    if (tid < DH) {
      float s = 0.f;
#pragma unroll
      for (int w = 0; w < 8; ++w) s += red[w * DH + tid];
      st_g(rs, I_CTX + (h * 2 + half) * DH + tid, s, 0.f, tb);
      float v[1];
      gather<1>(rs, I_CTX + (h * 2 + (half ^ 1)) * DH + tid, 0, tb, v);
      ((LAS float*)(L + L_VEC))[tid] = rbf(half ? v[0] + s : s + v[0]);
    }
    WGBAR();
    STAMP(10);
    float a8[8];
#pragma unroll
    for (int k = 0; k < 8; ++k) a8[k] = 0.f;
#pragma unroll
    for (int i = 0; i < 24; ++i) {
      const float x = cv[24 * wave + i];
#pragma unroll
      for (int k = 0; k < 4; ++k) { a8[2 * k] = fmaf(x, bflo(wo[i][k]), a8[2 * k]); a8[2 * k + 1] = fmaf(x, bfhi(wo[i][k]), a8[2 * k + 1]); }
    }
    LAS float* r2 = (LAS float*)(L + L_BIG);
    if (lane2 < 40) {
#pragma unroll
      for (int k = 0; k < 8; ++k) r2[wave * 320 + 8 * lane2 + k] = a8[k];
    }
    WGBAR();
    if (tid < 320) {
      float s = 0.f;
#pragma unroll
      for (int w = 0; w < 8; ++w) s += r2[w * 320 + tid];
      st_g(rs, I_OUT + (NH + h) * DM + 320 * half + tid, s, 0.f, tb);
    }
    STAMP(11);
    if ((kb_ % 12) == 3 && kb_ < 60) out_reduce(rs, ws, out, L, 1, 128 * (kb_ / 12), tid, tb);
    STAMP(12);
  }
}

__global__ void __launch_bounds__(512, 2) mega(const u16* __restrict__ xA, const u16* __restrict__ K, const u16* __restrict__ V,
                                               const u16* __restrict__ wqA, const u16* __restrict__ woA,
                                               const u16* __restrict__ xB, const u16* __restrict__ ckv, const u16* __restrict__ wqB,
                                               const u16* __restrict__ wuk, const u16* __restrict__ wuv, const u16* __restrict__ woB,
                                               unsigned char* ws, u16* __restrict__ out) {
  extern __shared__ __attribute__((aligned(16))) unsigned char smem[];
  LAS unsigned char* L = (LAS unsigned char*)smem;
  const int tid = threadIdx.x, lane = tid & 63;
  const int wave = __builtin_amdgcn_readfirstlane(tid >> 6);
  const int b = blockIdx.x;
  const int h = (b & 7) * 4 + (b >> 6), jj = (b >> 3) & 7;
  if (jj < NA) a_role(xA, wqA, xB, wqB, K, V, woA, ws, out, L, h, jj, wave, lane);
  else b_role(xA, wqA, xB, wqB, ckv, wuk, wuv, woB, ws, out, L, h, jj - NA, wave, lane);
}

extern "C" void kernel_launch(void* const* d_in, const int* in_sizes, int n_in,
                              void* d_out, int out_size, void* d_ws, size_t ws_size,
                              hipStream_t stream) {
  (void)hipFuncSetAttribute((const void*)mega, hipFuncAttributeMaxDynamicSharedMemorySize, L_BYTES);
  mega<<<256, 512, L_BYTES, stream>>>((const u16*)d_in[0], (const u16*)d_in[1], (const u16*)d_in[2], (const u16*)d_in[3], (const u16*)d_in[4],
                                      (const u16*)d_in[5], (const u16*)d_in[6], (const u16*)d_in[7], (const u16*)d_in[8], (const u16*)d_in[9],
                                      (const u16*)d_in[10], (unsigned char*)d_ws, (u16*)d_out);
}
```

```cpp
#include <hip/hip_runtime.h>
#include <hip/hip_bf16.h>
#include <stdint.h>

typedef unsigned u32x4 __attribute__((ext_vector_type(4)));
typedef unsigned u32x2 __attribute__((ext_vector_type(2)));
typedef __bf16 bf16x2 __attribute__((ext_vector_type(2)));
typedef __bf16 bf16x8 __attribute__((ext_vector_type(8)));
typedef float f32x16 __attribute__((ext_vector_type(16)));
typedef float f32x4 __attribute__((ext_vector_type(4)));
typedef unsigned short u16;
typedef unsigned long long u64;

#define DI __device__ __forceinline__
#define GAS __attribute__((address_space(1)))
#define LAS __attribute__((address_space(3)))
#define RLX_AGENT __ATOMIC_RELAXED, __HIP_MEMORY_SCOPE_AGENT

constexpr int NH = 32, SK = 8192, DH = 192, DM = 640, DC = 768, HD = NH * DH;
constexpr float SCALE = 0.07216878364870322f;
constexpr float LOG2E = 1.4426950408889634f;
#define A_KR 2
#define A_NST 19
#define EARLY_W 3
#define POLL_SLEEP 4
constexpr int NA = 6;
constexpr int NSB = 64;
constexpr unsigned kSpinCap = 1u << 18;

constexpr size_t Z_GENA = 0, Z_GENB = 128;
constexpr size_t Z_FQABS = 1024;
constexpr size_t Z_FBATT = 2048;
constexpr size_t Z_TCNT = 4096;
constexpr size_t Z_GRAN = 8192;
constexpr int I_QP = 0;
constexpr int I_CTX = I_QP + NH * 5 * 384;
constexpr int I_AP = I_CTX + NH * 2 * 192;
constexpr int I_OUT = I_AP + NH * NA * 200;
constexpr int I_END = I_OUT + 2 * NH * DM;
constexpr size_t W_QH = Z_GRAN + (size_t)I_END * 16;
constexpr size_t W_QL = W_QH + (size_t)NH * DC * 2;
constexpr size_t W_BML = W_QL + (size_t)NH * DC * 2;
constexpr size_t W_BO = W_BML + (size_t)NH * NSB * 2 * 4;
constexpr size_t W_END = W_BO + (size_t)NSB * NH * DC * 2;
static_assert(W_END < (1ull << 31), "ws layout");

constexpr int L_QS = 0;
constexpr int L_VEC = 512;
constexpr int L_RED = 2048;
constexpr int L_BIG = 16384;
constexpr int L_SF = L_BIG + 131072;
constexpr int L_BYTES = L_SF + 16384;

DI float dot2(unsigned a, unsigned b, float c) {
  return __builtin_amdgcn_fdot2_f32_bf16(__builtin_bit_cast(bf16x2, a), __builtin_bit_cast(bf16x2, b), c, false);
}
DI float rbf(float x) { return (float)(__bf16)x; }
DI float asf(unsigned v) { return __builtin_bit_cast(float, v); }
DI unsigned asu(float v) { return __builtin_bit_cast(unsigned, v); }
DI float bflo(unsigned v) { return __builtin_bit_cast(float, v << 16); }
DI float bfhi(unsigned v) { return __builtin_bit_cast(float, v & 0xffff0000u); }
DI u16 f2bf(float x) { return __builtin_bit_cast(u16, (__bf16)x); }
DI unsigned pack2(float lo, float hi) { return (unsigned)f2bf(lo) | ((unsigned)f2bf(hi) << 16); }
template <int CTRL> DI float dpp(float x) {
  return __builtin_bit_cast(float, __builtin_amdgcn_mov_dpp(__builtin_bit_cast(int, x), CTRL, 0xf, 0xf, true));
}
DI float row_sum16(float x) {
  x += dpp<0x128>(x); x += dpp<0x124>(x); x += dpp<0x122>(x); x += dpp<0x121>(x); return x;
}
DI float ex2(float x) { return __builtin_amdgcn_exp2f(x); }
template <typename T> DI T ntload(const void* p) { return __builtin_nontemporal_load(reinterpret_cast<const T*>(p)); }
#define MFMA32(a, b, c) __builtin_amdgcn_mfma_f32_32x32x16_bf16(__builtin_bit_cast(bf16x8, a), __builtin_bit_cast(bf16x8, b), (c), 0, 0, 0)
DI u32x4 pack8(const f32x16& x, int s) {
  u32x4 p;
#pragma unroll
  for (int j = 0; j < 4; ++j) p[j] = pack2(x[8 * s + 2 * j], x[8 * s + 2 * j + 1]);
  return p;
}
DI int opaque(int x) { asm volatile("" : "+v"(x)); return x; }
#define VM0() asm volatile("s_waitcnt vmcnt(0)" ::: "memory")
#define CBAR() asm volatile("" ::: "memory")
#define WGBAR() do { asm volatile("s_waitcnt lgkmcnt(0)" ::: "memory"); __builtin_amdgcn_s_barrier(); asm volatile("" ::: "memory"); } while (0)

#define STAMP(k) do { } while (0)

typedef __amdgpu_buffer_rsrc_t rsrc_t;
struct Tag { unsigned lo, hi; };
DI Tag load_tag(unsigned char* ws, size_t off) {
  const u64 g = __hip_atomic_load((GAS u64*)(ws + off), RLX_AGENT) + 1ull;
  Tag t;
  t.lo = __builtin_amdgcn_readfirstlane((unsigned)g);
  t.hi = __builtin_amdgcn_readfirstlane((unsigned)(g >> 32));
  return t;
}
DI void st_g(rsrc_t rs, int idx, float v0, float v1, Tag t) {
  const u32x4 x = {asu(v0), asu(v1), t.lo, t.hi};
  __builtin_amdgcn_raw_buffer_store_b128(x, rs, (int)Z_GRAN + idx * 16, 0, 16);
}
DI u32x4 ld_g(rsrc_t rs, int idx) { return __builtin_amdgcn_raw_buffer_load_b128(rs, (int)Z_GRAN + idx * 16, 0, 16); }
DI bool tag_ok(const u32x4& x, Tag t) { return (x[2] == t.lo) & (x[3] == t.hi); }
DI void st_flag(rsrc_t rs, int off, Tag t) {
  const u32x2 x = {t.lo, t.hi};
  __builtin_amdgcn_raw_buffer_store_b64(x, rs, off, 0, 16);
}
DI bool spin_fail(unsigned& spins) {
  __builtin_amdgcn_s_sleep(1);
  return ++spins > kSpinCap;
}
DI void prepoll(rsrc_t rs, int idx, int stride, int n, Tag t, int lane) {
  for (unsigned spins = 0;;) {
    bool ok = true;
    if (lane < n) ok = tag_ok(ld_g(rs, idx + lane * stride), t);
    if (__all(ok)) break;
    __builtin_amdgcn_s_sleep(POLL_SLEEP);
    if (++spins > kSpinCap) break;
  }
  CBAR();
}
template <int N> DI void gather(rsrc_t rs, int idx, int stride, Tag t, float (&v)[N]) {
  for (unsigned spins = 0;;) {
    bool ok = true;
#pragma unroll
    for (int k = 0; k < N; ++k) {
      const u32x4 x = ld_g(rs, idx + k * stride);
      v[k] = asf(x[0]);
      ok &= tag_ok(x, t);
    }
    if (__all(ok)) break;
    if (spin_fail(spins)) break;
  }
  CBAR();
}

DI void fma_row(float (&acc)[12], float p, const u32x4& a, const u32x2& b) {
#pragma unroll
  for (int k = 0; k < 4; ++k) { acc[2 * k] = fmaf(p, bflo(a[k]), acc[2 * k]); acc[2 * k + 1] = fmaf(p, bfhi(a[k]), acc[2 * k + 1]); }
#pragma unroll
  for (int k = 0; k < 2; ++k) { acc[8 + 2 * k] = fmaf(p, bflo(b[k]), acc[8 + 2 * k]); acc[8 + 2 * k + 1] = fmaf(p, bfhi(b[k]), acc[8 + 2 * k + 1]); }
}
DI void rows_to_lds(float (&acc)[12], LAS float* dst, int lane) {
  const int r = lane >> 4, c = lane & 15;
#pragma unroll
  for (int k = 0; k < 12; ++k) { float a = acc[k]; a += __shfl_xor(a, 16); a += __shfl_xor(a, 32); acc[k] = a; }
  if (r == 0) {
#pragma unroll
    for (int k = 0; k < 8; ++k) dst[8 * c + k] = acc[k];
#pragma unroll
    for (int k = 0; k < 4; ++k) dst[128 + 4 * c + k] = acc[8 + k];
  }
}

DI void out_reduce(rsrc_t rs, unsigned char* ws, u16* __restrict__ out, LAS unsigned char* L, int mat, int col0, int tid, Tag t) {
  const int col = tid & 127, qd = tid >> 7;
  float v[8];
  gather<8>(rs, I_OUT + (mat * NH + qd * 8) * DM + col0 + col, DM, t, v);
  LAS float* red = (LAS float*)(L + L_RED);
  red[tid] = ((v[0] + v[1]) + (v[2] + v[3])) + ((v[4] + v[5]) + (v[6] + v[7]));
  WGBAR();
  if (tid < 128) out[mat * DM + col0 + tid] = f2bf((red[tid] + red[128 + tid]) + (red[256 + tid] + red[384 + tid]));
  if (col0 == 0 && tid == 0) st_flag(rs, (int)(mat ? Z_GENB : Z_GENA), t);
}

constexpr int NQ = 5;
struct QLoads { u32x4 a[8]; u32x2 b[8]; float x[8]; };
DI void q_issue(QLoads& Q, const u16* __restrict__ xA, const u16* __restrict__ wqA, const u16* __restrict__ xB,
                const u16* __restrict__ wqB, int h, int jj, int wave, int lane) {
  const int r = lane >> 4, c = lane & 15;
  const u16* x = wave < 4 ? xA : xB;
  const u16* w = wave < 4 ? wqA : wqB;
#pragma unroll
  for (int i = 0; i < 8; ++i) {
    const int row = 128 * jj + 32 * (wave & 3) + 4 * i + r;
    const u16* p = w + (size_t)row * HD + h * DH;
    Q.a[i] = ntload<u32x4>(p + 8 * c); Q.b[i] = ntload<u32x2>(p + 128 + 4 * c);
    Q.x[i] = bflo((unsigned)x[row]);
  }
}
DI void q_partial(QLoads& Q, LAS unsigned char* L, int wave, int lane) {
  float acc[12];
#pragma unroll
  for (int k = 0; k < 12; ++k) acc[k] = 0.f;
#pragma unroll
  for (int i = 0; i < 8; ++i) fma_row(acc, Q.x[i], Q.a[i], Q.b[i]);
  rows_to_lds(acc, (LAS float*)(L + L_RED) + wave * DH, lane);
}
DI void q_publish(rsrc_t rs, LAS unsigned char* L, int h, int jj, int wave, int lane, Tag ta, Tag tb) {
  const int tid = wave * 64 + lane;
  const LAS float* red = (const LAS float*)(L + L_RED);
  if (wave < 6) {
    const int mt = wave >= 3 ? 1 : 0, d = tid - mt * DH;
    const LAS float* rp = red + mt * 4 * DH + d;
    st_g(rs, I_QP + (h * NQ + jj) * 384 + tid, (rp[0] + rp[DH]) + (rp[2 * DH] + rp[3 * DH]), 0.f, mt ? tb : ta);
  }
}
DI void q_gather(rsrc_t rs, LAS unsigned char* L, int h, int part, int wave, int lane, Tag t) {
  const int tid = wave * 64 + lane;
  if (wave < 3) {
    float v[NQ];
    prepoll(rs, I_QP + h * NQ * 384 + part * DH + wave * 64 + 63, 384, NQ, t, lane);
    gather<NQ>(rs, I_QP + h * NQ * 384 + part * DH + tid, 384, t, v);
    ((LAS u16*)(L + L_QS))[tid] = f2bf(((v[0] + v[1]) + (v[2] + v[3])) + v[4]);
  }
}

DI void a_role(const u16* __restrict__ xA, const u16* __restrict__ wqA, const u16* __restrict__ xB, const u16* __restrict__ wqB,
               const u16* __restrict__ K, const u16* __restrict__ V, const u16* __restrict__ woA,
               unsigned char* ws, u16* __restrict__ out, LAS unsigned char* L, int h, int j, int wave, int lane) {
  const int r = lane >> 4, c = lane & 15, tid = wave * 64 + lane;
  STAMP(0);
  const rsrc_t rs = __builtin_amdgcn_make_buffer_rsrc((void*)ws, 0, (int)W_END, 0x00020000);
  const Tag ta = load_tag(ws, Z_GENA), tb = load_tag(ws, Z_GENB);
  QLoads Q;
  if (j < NQ) q_issue(Q, xA, wqA, xB, wqB, h, j, wave, lane);
  constexpr int KR = A_KR, TK = 4 * KR, NTH = SK / TK;
  constexpr int NST = A_NST, DYN0 = 48 * NST;
  static_assert(NST >= 2 && DYN0 <= NTH, "tile split");
  const int gw = j * 8 + wave;
  const int t_beg = gw * NST;
  const size_t base = ((size_t)h * SK + r) * DH;
  const u16* kp = K + base;
  const u16* vp = V + base;
  u32x4 ka[KR], va[KR]; u32x2 kb[KR], vb[KR];
#define LOADT(t) do { _Pragma("unroll") for (int i = 0; i < KR; ++i) { \
    const u16* kr = kp + (size_t)((t) * TK + i * 4) * DH; const u16* vr = vp + (size_t)((t) * TK + i * 4) * DH; \
    ka[i] = ntload<u32x4>(kr + 8 * c); kb[i] = ntload<u32x2>(kr + 128 + 4 * c); \
    va[i] = ntload<u32x4>(vr + 8 * c); vb[i] = ntload<u32x2>(vr + 128 + 4 * c); } } while (0)
  GAS unsigned* tcnt = (GAS unsigned*)(ws + Z_TCNT) + h * 32;
  if (j == 0 && tid == 0) __hip_atomic_store(tcnt, 0u, RLX_AGENT);
  if (j < NQ) q_partial(Q, L, wave, lane);
  if (j == 0 && wave == 0) VM0();
  STAMP(10);
  WGBAR();
  STAMP(11);
  __builtin_amdgcn_sched_barrier(0);
  if (wave >= EARLY_W) LOADT(t_beg);
  __builtin_amdgcn_sched_barrier(0);
  if (j < NQ) q_publish(rs, L, h, j, wave, lane, ta, tb);
  q_gather(rs, L, h, 0, wave, lane, ta);
  STAMP(12);
  __builtin_amdgcn_sched_barrier(0);
  if (wave < EARLY_W) LOADT(t_beg);
  __builtin_amdgcn_sched_barrier(0);
  WGBAR();
  STAMP(1);
  const u32x4 qa = *(const LAS u32x4*)(L + L_QS + 16 * c);
  const u32x2 qb = *(const LAS u32x2*)(L + L_QS + 256 + 8 * c);
  float m = -INFINITY, l = 0.f, acc[12];
#pragma unroll
  for (int k = 0; k < 12; ++k) acc[k] = 0.f;
  unsigned nx = 0u;
  for (int i = 0;; ++i) {
    const int t_next = (i + 1 < NST) ? t_beg + i + 1 : DYN0 + __builtin_amdgcn_readfirstlane((int)nx);
    if (i + 2 >= NST && lane == 0) nx = __hip_atomic_fetch_add(tcnt, 1u, RLX_AGENT);
    __builtin_amdgcn_sched_barrier(0);
    {
      float s[KR];
      float mn = m;
#pragma unroll
      for (int q = 0; q < KR; ++q) {
        float d = 0.f;
#pragma unroll
        for (int k = 0; k < 4; ++k) d = dot2(ka[q][k], qa[k], d);
        d = dot2(kb[q][0], qb[0], d); d = dot2(kb[q][1], qb[1], d);
        d = row_sum16(d);
        s[q] = rbf(rbf(d) * SCALE);
        mn = fmaxf(mn, s[q]);
      }
      const float alpha = ex2((m - mn) * LOG2E);
      m = mn;
      float ps = 0.f;
#pragma unroll
      for (int q = 0; q < KR; ++q) { s[q] = ex2((s[q] - mn) * LOG2E); ps += s[q]; }
      l = l * alpha + ps;
#pragma unroll
      for (int k = 0; k < 12; ++k) acc[k] *= alpha;
#pragma unroll
      for (int q = 0; q < KR; ++q) fma_row(acc, s[q], va[q], vb[q]);
    }
    if (t_next >= NTH) break;
    LOADT(t_next);
  }
#undef LOADT
  STAMP(2);
  u32x4 wv[6];
  if (j < 5) {
#pragma unroll
    for (int i = 0; i < 6; ++i) wv[i] = ntload<u32x4>(woA + (size_t)(h * DH + 24 * wave + 4 * i + r) * DM + 128 * j + 8 * c);
  }
  float M = fmaxf(m, __shfl_xor(m, 16)); M = fmaxf(M, __shfl_xor(M, 32));
  const float f = ex2((m - M) * LOG2E);
  l *= f; l += __shfl_xor(l, 16); l += __shfl_xor(l, 32);
#pragma unroll
  for (int k = 0; k < 12; ++k) acc[k] *= f;
  LAS float* red = (LAS float*)(L + L_RED);
  rows_to_lds(acc, red + wave * 200 + 8, lane);
  if (lane == 0) { red[wave * 200] = M; red[wave * 200 + 1] = l; }
  WGBAR();
  {
    float mw[8], MM = -INFINITY;
#pragma unroll
    for (int w = 0; w < 8; ++w) { mw[w] = red[w * 200]; MM = fmaxf(MM, mw[w]); }
    const int idx = tid < DH ? 8 + tid : 1;
    float s = 0.f;
#pragma unroll
    for (int w = 0; w < 8; ++w) s = fmaf(ex2((mw[w] - MM) * LOG2E), red[w * 200 + idx], s);
    const int ga = I_AP + (h * NA + j) * 200;
    if (tid < DH) st_g(rs, ga + 8 + tid, s, 0.f, ta);
    if (tid == DH) st_g(rs, ga, MM, s, ta);
  }
  if (j < 5) {
    if (wave < 3) {
      float v[NA], mm[NA], ll[NA];
      const int gh = I_AP + h * NA * 200;
      for (unsigned spins = 0;;) {
        bool ok = true;
#pragma unroll
        for (int k = 0; k < NA; ++k) {
          const u32x4 x = ld_g(rs, gh + k * 200 + 8 + tid), y = ld_g(rs, gh + k * 200);
          v[k] = asf(x[0]); mm[k] = asf(y[0]); ll[k] = asf(y[1]);
          ok &= tag_ok(x, ta) & tag_ok(y, ta);
        }
        if (__all(ok)) break;
        if (spin_fail(spins)) break;
      }
      CBAR();
      float Mx = mm[0];
#pragma unroll
      for (int k = 1; k < NA; ++k) Mx = fmaxf(Mx, mm[k]);
      float Lsum = 0.f, o = 0.f;
#pragma unroll
      for (int k = 0; k < NA; ++k) { const float g = ex2((mm[k] - Mx) * LOG2E); Lsum = fmaf(g, ll[k], Lsum); o = fmaf(g, v[k], o); }
      ((LAS float*)(L + L_VEC))[tid] = rbf(o / Lsum);
    }
    WGBAR();
    STAMP(3);
    const LAS float* vec = (const LAS float*)(L + L_VEC);
    float a8[8];
#pragma unroll
    for (int k = 0; k < 8; ++k) a8[k] = 0.f;
#pragma unroll
    for (int i = 0; i < 6; ++i) {
      const float x = vec[24 * wave + 4 * i + r];
#pragma unroll
      for (int k = 0; k < 4; ++k) { a8[2 * k] = fmaf(x, bflo(wv[i][k]), a8[2 * k]); a8[2 * k + 1] = fmaf(x, bfhi(wv[i][k]), a8[2 * k + 1]); }
    }
#pragma unroll
    for (int k = 0; k < 8; ++k) { a8[k] += __shfl_xor(a8[k], 16); a8[k] += __shfl_xor(a8[k], 32); }
    LAS float* r2 = (LAS float*)(L + L_BIG);
    if (r == 0) {
#pragma unroll
      for (int k = 0; k < 8; ++k) r2[wave * 128 + 8 * c + k] = a8[k];
    }
    WGBAR();
    if (tid < 128) {
      float s = 0.f;
#pragma unroll
      for (int w = 0; w < 8; ++w) s += r2[w * 128 + tid];
      st_g(rs, I_OUT + h * DM + 128 * j + tid, s, 0.f, ta);
    }
    STAMP(4);
    if (h == 4 * j + 1) out_reduce(rs, ws, out, L, 0, 128 * j, tid, ta);
    STAMP(5);
  }
}

DI void b_role(const u16* __restrict__ xA, const u16* __restrict__ wqA, const u16* __restrict__ xB, const u16* __restrict__ wqB,
               const u16* __restrict__ ckv, const u16* __restrict__ wuk, const u16* __restrict__ wuv, const u16* __restrict__ woB,
               unsigned char* ws, u16* __restrict__ out, LAS unsigned char* L, int h, int half, int wave, int lane) {
  const int r = lane >> 4, c = lane & 15, tid = wave * 64 + lane;
  const int cl = lane & 31, hh = lane >> 5;
  const int kb_ = 2 * h + half;
  const rsrc_t rs = __builtin_amdgcn_make_buffer_rsrc((void*)ws, 0, (int)W_END, 0x00020000);
  STAMP(0);
  const Tag tb = load_tag(ws, Z_GENB);
  u32x4 cf[4][6];
  {
    u32x4 ua[12]; u32x2 ub[12];
#define LOADU() do { _Pragma("unroll") for (int i = 0; i < 12; ++i) { \
      const u16* row = wuk + ((size_t)(384 * half + 48 * wave + 4 * i + r) * NH + h) * DH; \
      ua[i] = ntload<u32x4>(row + 8 * c); ub[i] = ntload<u32x2>(row + 128 + 4 * c); } } while (0)
#define LOADC() do { _Pragma("unroll") for (int t = 0; t < 4; ++t) _Pragma("unroll") for (int i = 0; i < 6; ++i) \
        cf[t][i] = ntload<u32x4>(ckv + (size_t)(128 * kb_ + 32 * t) * DC + (size_t)(384 * wave + 64 * i + lane) * 8); } while (0)
    LOADU();
    __builtin_amdgcn_sched_barrier(0);
    if (wave >= 3) LOADC();
    __builtin_amdgcn_sched_barrier(0);
    q_gather(rs, L, h, 1, wave, lane, tb);
    STAMP(15);
    __builtin_amdgcn_sched_barrier(0);
    if (wave == 2) LOADC();
    __builtin_amdgcn_sched_barrier(0);
    WGBAR();
    STAMP(1);
    const u32x4 qa = *(const LAS u32x4*)(L + L_QS + 16 * c);
    const u32x2 qb = *(const LAS u32x2*)(L + L_QS + 256 + 8 * c);
    LAS float* qv = (LAS float*)(L + L_VEC);
#pragma unroll
    for (int i = 0; i < 12; ++i) {
      float s = 0.f;
#pragma unroll
      for (int k = 0; k < 4; ++k) s = dot2(ua[i][k], qa[k], s);
      s = dot2(ub[i][0], qb[0], s); s = dot2(ub[i][1], qb[1], s);
      s = row_sum16(s);
      if (c == 0) qv[48 * wave + 4 * i + r] = s;
    }
    WGBAR();
    if (wave < 2) {
      if (lane < 48) {
        u32x4 o;
#pragma unroll
        for (int k = 0; k < 4; ++k) {
          const float a = qv[8 * lane + 2 * k], b = qv[8 * lane + 2 * k + 1];
          const float ah = rbf(a), bh = rbf(b);
          o[k] = wave ? pack2(a - ah, b - bh) : pack2(ah, bh);
        }
        const int c8 = 48 * half + lane;
        __builtin_amdgcn_raw_buffer_store_b128(o, rs, (int)(wave ? W_QL : W_QH) + ((c8 >> 1) * 64 + h + 32 * (c8 & 1)) * 16, 0, 16);
      }
      VM0();
      if (lane == 0) st_flag(rs, (int)Z_FQABS + (2 * kb_ + wave) * 8, tb);
      __builtin_amdgcn_sched_barrier(0);
      LOADC();
    }
#undef LOADC
#undef LOADU
  }
  STAMP(2);
#pragma unroll
  for (int rd = 0; rd < 2; ++rd) {
#pragma unroll
    for (int tt = 0; tt < 2; ++tt)
#pragma unroll
      for (int i = 0; i < 6; ++i) {
        const int e = 384 * wave + 64 * i + lane;
        *(LAS u32x4*)(L + L_BIG + tt * 49664 + (e / 96) * 1552 + (e % 96) * 16) = cf[2 * rd + tt][i];
      }
    WGBAR();
#pragma unroll
    for (int tt = 0; tt < 2; ++tt)
#pragma unroll
      for (int s = 0; s < 6; ++s)
        cf[2 * rd + tt][s] = *(const LAS u32x4*)(L + L_BIG + tt * 49664 + cl * 1552 + (96 * wave + 16 * s + 8 * hh) * 2);
    WGBAR();
  }
  {
    if (wave == 0) {
      for (unsigned spins = 0;;) {
        const u32x4 f = __builtin_amdgcn_raw_buffer_load_b128(rs, (int)Z_FQABS + lane * 16, 0, 16);
        if (__all((f[0] == tb.lo) & (f[1] == tb.hi) & (f[2] == tb.lo) & (f[3] == tb.hi))) break;
        __builtin_amdgcn_s_sleep(POLL_SLEEP);
        if (++spins > kSpinCap) break;
      }
    }
    WGBAR();
    STAMP(3);
    u32x4 qh[6], ql[6];
#pragma unroll
    for (int s = 0; s < 6; ++s) {
      qh[s] = __builtin_amdgcn_raw_buffer_load_b128(rs, (int)W_QH + ((wave * 6 + s) * 64 + lane) * 16, 0, 16);
      ql[s] = __builtin_amdgcn_raw_buffer_load_b128(rs, (int)W_QL + ((wave * 6 + s) * 64 + lane) * 16, 0, 16);
    }
    __builtin_amdgcn_sched_barrier(0);
    f32x16 zero;
#pragma unroll
    for (int i = 0; i < 16; ++i) zero[i] = 0.f;
    LAS f32x4* sp = (LAS f32x4*)(L + L_BIG);
#pragma unroll
    for (int t = 0; t < 4; ++t) {
      f32x16 S = zero;
#pragma unroll
      for (int s = 0; s < 6; ++s) S = MFMA32(cf[t][s], ql[s], S);
#pragma unroll
      for (int s = 0; s < 6; ++s) S = MFMA32(cf[t][s], qh[s], S);
#pragma unroll
      for (int g = 0; g < 4; ++g) sp[((wave * 4 + t) * 4 + g) * 64 + lane] = f32x4{S[4 * g], S[4 * g + 1], S[4 * g + 2], S[4 * g + 3]};
    }
    STAMP(4);
    WGBAR();
    float sv[8];
    {
      const int t = wave >> 1;
#pragma unroll
      for (int gg = 0; gg < 2; ++gg) {
        const int g = 2 * (wave & 1) + gg;
        f32x4 a = sp[((0 * 4 + t) * 4 + g) * 64 + lane];
#pragma unroll
        for (int w = 1; w < 8; ++w) a += sp[((w * 4 + t) * 4 + g) * 64 + lane];
#pragma unroll
        for (int k = 0; k < 4; ++k) sv[4 * gg + k] = rbf(rbf(a[k]) * SCALE);
      }
    }
    float m = fmaxf(fmaxf(fmaxf(sv[0], sv[1]), fmaxf(sv[2], sv[3])), fmaxf(fmaxf(sv[4], sv[5]), fmaxf(sv[6], sv[7])));
    m = fmaxf(m, __shfl_xor(m, 32));
    LAS float* mx = (LAS float*)(L + L_RED);
    if (lane < 32) mx[wave * 32 + lane] = m;
    WGBAR();
#pragma unroll
    for (int w = 0; w < 8; ++w) m = fmaxf(m, mx[w * 32 + cl]);
    float l = 0.f;
#pragma unroll
    for (int k = 0; k < 8; ++k) { sv[k] = ex2((sv[k] - m) * LOG2E); l += sv[k]; }
    l += __shfl_xor(l, 32);
    if (lane < 32) mx[256 + wave * 32 + lane] = l;
    LAS u32x4* pf = (LAS u32x4*)(L + L_SF);
    pf[wave * 64 + lane] = u32x4{pack2(sv[0], sv[1]), pack2(sv[2], sv[3]), pack2(sv[4], sv[5]), pack2(sv[6], sv[7])};
    WGBAR();
    u32x4 pb[4][2];
#pragma unroll
    for (int t = 0; t < 4; ++t) { pb[t][0] = pf[(2 * t) * 64 + lane]; pb[t][1] = pf[(2 * t + 1) * 64 + lane]; }
    if (wave == 0 && lane < 32) {
      float ls = 0.f;
#pragma unroll
      for (int w = 0; w < 8; ++w) ls += mx[256 + w * 32 + lane];
      const u32x2 ml = {asu(m), asu(ls)};
      __builtin_amdgcn_raw_buffer_store_b64(ml, rs, (int)W_BML + ((cl * NSB + kb_) * 2) * 4, 0, 16);
    }
    u32x4 idlo, idhi;
#pragma unroll
    for (int k = 0; k < 4; ++k) {
      const int k0 = 8 * hh + 2 * k, k1 = k0 + 1;
      idlo[k] = (cl == k0 ? 0x3f80u : 0u) | (cl == k1 ? 0x3f800000u : 0u);
      idhi[k] = (cl == k0 + 16 ? 0x3f80u : 0u) | (cl == k1 + 16 ? 0x3f800000u : 0u);
    }
    STAMP(5);
    LAS float* ost = (LAS float*)(L + L_BIG) + wave * 3200;
#pragma unroll
    for (int ct = 0; ct < 3; ++ct) {
      f32x16 O = zero;
#pragma unroll
      for (int t = 0; t < 4; ++t) {
        f32x16 T = MFMA32(cf[t][2 * ct], idlo, zero);
        T = MFMA32(cf[t][2 * ct + 1], idhi, T);
        O = MFMA32(pack8(T, 0), pb[t][0], O);
        O = MFMA32(pack8(T, 1), pb[t][1], O);
      }
#pragma unroll
      for (int g = 0; g < 4; ++g)
        *(LAS f32x4*)(ost + cl * 100 + 32 * ct + 8 * g + 4 * hh) = f32x4{O[4 * g], O[4 * g + 1], O[4 * g + 2], O[4 * g + 3]};
    }
    asm volatile("s_waitcnt lgkmcnt(0)" ::: "memory");
#pragma unroll
    for (int i = 0; i < 6; ++i) {
      const int e = i * 64 + lane, hd = e / 12, q8 = e % 12;
      const f32x4 v0 = *(const LAS f32x4*)(ost + hd * 100 + 8 * q8), v1 = *(const LAS f32x4*)(ost + hd * 100 + 8 * q8 + 4);
      const u32x4 o = {pack2(v0[0], v0[1]), pack2(v0[2], v0[3]), pack2(v1[0], v1[1]), pack2(v1[2], v1[3])};
      __builtin_amdgcn_raw_buffer_store_b128(o, rs, (int)W_BO + ((kb_ * NH + hd) * DC + 96 * wave + 8 * q8) * 2, 0, 16);
    }
    STAMP(6);
    VM0();
    WGBAR();
    if (tid == 0) st_flag(rs, (int)Z_FBATT + kb_ * 8, tb);
    STAMP(7);
  }
  {
    const int lane2 = opaque(lane);
    const int r = lane2 >> 4, c = lane2 & 15, tid = wave * 64 + lane2;
    if (wave != 0) {
      __builtin_amdgcn_sched_barrier(0);
    }
    u32x4 va[12]; u32x2 vb[12];
#pragma unroll
    for (int i = 0; i < 12; ++i) {
      const u16* row = wuv + ((size_t)(384 * half + 48 * wave + 4 * i + r) * NH + h) * DH;
      va[i] = ntload<u32x4>(row + 8 * c); vb[i] = ntload<u32x2>(row + 128 + 4 * c);
    }
    __builtin_amdgcn_sched_barrier(0);
    if (wave == 0) {
      for (unsigned spins = 0;;) {
        const u32x2 f = __builtin_amdgcn_raw_buffer_load_b64(rs, (int)Z_FBATT + lane2 * 8, 0, 16);
        if (__all((f[0] == tb.lo) & (f[1] == tb.hi))) break;
        __builtin_amdgcn_s_sleep(POLL_SLEEP);
        if (++spins > kSpinCap) break;
      }
    }
    WGBAR();
    STAMP(8);
    const u32x2 st = __builtin_amdgcn_raw_buffer_load_b64(rs, (int)W_BML + (h * NSB + lane2) * 8, 0, 16);
    u32x4 pa[8];
    const int l48 = lane2 < 48 ? lane2 : 0;
#pragma unroll
    for (int i = 0; i < 8; ++i)
      pa[i] = __builtin_amdgcn_raw_buffer_load_b128(rs, (int)W_BO + (((8 * wave + i) * NH + h) * DC + 384 * half + 8 * l48) * 2, 0, 16);
    const float m0 = asf(st[0]), l0 = asf(st[1]);
    float M = m0;
#pragma unroll
    for (int o = 1; o < 64; o <<= 1) M = fmaxf(M, __shfl_xor(M, o));
    const float f0 = ex2((m0 - M) * LOG2E);
    float Ls = f0 * l0;
#pragma unroll
    for (int o = 1; o < 64; o <<= 1) Ls += __shfl_xor(Ls, o);
    const float w0 = f0 / Ls;
    float sa[8];
#pragma unroll
    for (int k = 0; k < 8; ++k) sa[k] = 0.f;
#pragma unroll
    for (int i = 0; i < 8; ++i) {
      const float w = __shfl(w0, 8 * wave + i);
#pragma unroll
      for (int k = 0; k < 4; ++k) { sa[2 * k] = fmaf(w, bflo(pa[i][k]), sa[2 * k]); sa[2 * k + 1] = fmaf(w, bfhi(pa[i][k]), sa[2 * k + 1]); }
    }
    LAS f32x4* acc4 = (LAS f32x4*)(L + L_RED);
    if (lane2 < 48) {
      acc4[wave * 96 + 2 * lane2] = f32x4{sa[0], sa[1], sa[2], sa[3]};
      acc4[wave * 96 + 2 * lane2 + 1] = f32x4{sa[4], sa[5], sa[6], sa[7]};
    }
    __builtin_amdgcn_sched_barrier(0);
    u32x4 wo[24];
    const int lc = lane2 < 40 ? lane2 : 0;
#pragma unroll
    for (int i = 0; i < 24; ++i) wo[i] = ntload<u32x4>(woB + (size_t)(h * DH + 24 * wave + i) * DM + 320 * half + 8 * lc);
    __builtin_amdgcn_sched_barrier(0);
    WGBAR();
    if (tid < 384) {
      const LAS float* af = (const LAS float*)(L + L_RED) + tid;
      float s = 0.f;
#pragma unroll
      for (int w = 0; w < 8; ++w) s += af[w * 384];
      ((LAS float*)(L + L_VEC))[tid] = s;
    }
    WGBAR();
    STAMP(9);
    float acc[12];
#pragma unroll
    for (int k = 0; k < 12; ++k) acc[k] = 0.f;
    const LAS float* cv = (const LAS float*)(L + L_VEC);
#pragma unroll
    for (int i = 0; i < 12; ++i) fma_row(acc, cv[48 * wave + 4 * i + r], va[i], vb[i]);
    LAS float* red = (LAS float*)(L + L_RED);
    rows_to_lds(acc, red + wave * DH, lane2);
    WGBAR();
    if (tid < DH) {
      float s = 0.f;
#pragma unroll
      for (int w = 0; w < 8; ++w) s += red[w * DH + tid];
      st_g(rs, I_CTX + (h * 2 + half) * DH + tid, s, 0.f, tb);
      float v[1];
      gather<1>(rs, I_CTX + (h * 2 + (half ^ 1)) * DH + tid, 0, tb, v);
      ((LAS float*)(L + L_VEC))[tid] = rbf(half ? v[0] + s : s + v[0]);
    }
    WGBAR();
    STAMP(10);
    float a8[8];
#pragma unroll
    for (int k = 0; k < 8; ++k) a8[k] = 0.f;
#pragma unroll
    for (int i = 0; i < 24; ++i) {
      const float x = cv[24 * wave + i];
#pragma unroll
      for (int k = 0; k < 4; ++k) { a8[2 * k] = fmaf(x, bflo(wo[i][k]), a8[2 * k]); a8[2 * k + 1] = fmaf(x, bfhi(wo[i][k]), a8[2 * k + 1]); }
    }
    LAS float* r2 = (LAS float*)(L + L_BIG);
    if (lane2 < 40) {
#pragma unroll
      for (int k = 0; k < 8; ++k) r2[wave * 320 + 8 * lane2 + k] = a8[k];
    }
    WGBAR();
    if (tid < 320) {
      float s = 0.f;
#pragma unroll
      for (int w = 0; w < 8; ++w) s += r2[w * 320 + tid];
      st_g(rs, I_OUT + (NH + h) * DM + 320 * half + tid, s, 0.f, tb);
    }
    STAMP(11);
    if ((kb_ % 12) == 3 && kb_ < 60) out_reduce(rs, ws, out, L, 1, 128 * (kb_ / 12), tid, tb);
    STAMP(12);
  }
}

__global__ void __launch_bounds__(512, 2) mega(const u16* __restrict__ xA, const u16* __restrict__ K, const u16* __restrict__ V,
                                               const u16* __restrict__ wqA, const u16* __restrict__ woA,
                                               const u16* __restrict__ xB, const u16* __restrict__ ckv, const u16* __restrict__ wqB,
                                               const u16* __restrict__ wuk, const u16* __restrict__ wuv, const u16* __restrict__ woB,
                                               unsigned char* ws, u16* __restrict__ out) {
  extern __shared__ __attribute__((aligned(16))) unsigned char smem[];
  LAS unsigned char* L = (LAS unsigned char*)smem;
  const int tid = threadIdx.x, lane = tid & 63;
  const int wave = __builtin_amdgcn_readfirstlane(tid >> 6);
  const int b = blockIdx.x;
  const int h = b >> 3, jj = (b + (b >> 3)) & 7;
  if (jj < NA) a_role(xA, wqA, xB, wqB, K, V, woA, ws, out, L, h, jj, wave, lane);
  else b_role(xA, wqA, xB, wqB, ckv, wuk, wuv, woB, ws, out, L, h, jj - NA, wave, lane);
}

extern "C" void kernel_launch(void* const* d_in, const int* in_sizes, int n_in,
                              void* d_out, int out_size, void* d_ws, size_t ws_size,
                              hipStream_t stream) {
  (void)hipFuncSetAttribute((const void*)mega, hipFuncAttributeMaxDynamicSharedMemorySize, L_BYTES);
  mega<<<256, 512, L_BYTES, stream>>>((const u16*)d_in[0], (const u16*)d_in[1], (const u16*)d_in[2], (const u16*)d_in[3], (const u16*)d_in[4],
                                      (const u16*)d_in[5], (const u16*)d_in[6], (const u16*)d_in[7], (const u16*)d_in[8], (const u16*)d_in[9],
                                      (const u16*)d_in[10], (unsigned char*)d_ws, (u16*)d_out);
}
```

```cpp
#include <hip/hip_runtime.h>
#include <hip/hip_bf16.h>
#include <stdint.h>

typedef unsigned u32x4 __attribute__((ext_vector_type(4)));
typedef unsigned u32x2 __attribute__((ext_vector_type(2)));
typedef __bf16 bf16x2 __attribute__((ext_vector_type(2)));
typedef __bf16 bf16x8 __attribute__((ext_vector_type(8)));
typedef float f32x16 __attribute__((ext_vector_type(16)));
typedef float f32x4 __attribute__((ext_vector_type(4)));
typedef unsigned short u16;
typedef unsigned long long u64;

#define DI __device__ __forceinline__
#define GAS __attribute__((address_space(1)))
#define LAS __attribute__((address_space(3)))
#define RLX_AGENT __ATOMIC_RELAXED, __HIP_MEMORY_SCOPE_AGENT

constexpr int NH = 32, SK = 8192, DH = 192, DM = 640, DC = 768, HD = NH * DH;
constexpr float SCALE = 0.07216878364870322f;
constexpr float LOG2E = 1.4426950408889634f;
#define A_KR 2
#define A_NST 19
#define EARLY_W 3
#define POLL_SLEEP 4
constexpr int NA = 6;
constexpr int NSB = 64;
constexpr unsigned kSpinCap = 1u << 18;

constexpr size_t Z_GENA = 0, Z_GENB = 128;
constexpr size_t Z_FQABS = 1024;
constexpr size_t Z_FBATT = 2048;
constexpr size_t Z_TCNT = 4096;
constexpr size_t Z_GRAN = 8192;
constexpr int I_QP = 0;
constexpr int I_CTX = I_QP + NH * 5 * 384;
constexpr int I_AP = I_CTX + NH * 2 * 192;
constexpr int I_OUT = I_AP + NH * NA * 200;
constexpr int I_END = I_OUT + 2 * NH * DM;
constexpr size_t W_QH = Z_GRAN + (size_t)I_END * 16;
constexpr size_t W_QL = W_QH + (size_t)NH * DC * 2;
constexpr size_t W_BML = W_QL + (size_t)NH * DC * 2;
constexpr size_t W_BO = W_BML + (size_t)NH * NSB * 2 * 4;
constexpr size_t W_END = W_BO + (size_t)NSB * NH * DC * 2;
static_assert(W_END < (1ull << 31), "ws layout");

constexpr int L_QS = 0;
constexpr int L_VEC = 512;
constexpr int L_RED = 2048;
constexpr int L_BIG = 16384;
constexpr int L_SF = L_BIG + 131072;
constexpr int L_BYTES = L_SF + 16384;

DI float dot2(unsigned a, unsigned b, float c) {
  return __builtin_amdgcn_fdot2_f32_bf16(__builtin_bit_cast(bf16x2, a), __builtin_bit_cast(bf16x2, b), c, false);
}
DI float rbf(float x) { return (float)(__bf16)x; }
DI float asf(unsigned v) { return __builtin_bit_cast(float, v); }
DI unsigned asu(float v) { return __builtin_bit_cast(unsigned, v); }
DI float bflo(unsigned v) { return __builtin_bit_cast(float, v << 16); }
DI float bfhi(unsigned v) { return __builtin_bit_cast(float, v & 0xffff0000u); }
DI u16 f2bf(float x) { return __builtin_bit_cast(u16, (__bf16)x); }
DI unsigned pack2(float lo, float hi) { return (unsigned)f2bf(lo) | ((unsigned)f2bf(hi) << 16); }
template <int CTRL> DI float dpp(float x) {
  return __builtin_bit_cast(float, __builtin_amdgcn_mov_dpp(__builtin_bit_cast(int, x), CTRL, 0xf, 0xf, true));
}
DI float row_sum16(float x) {
  x += dpp<0x128>(x); x += dpp<0x124>(x); x += dpp<0x122>(x); x += dpp<0x121>(x); return x;
}
DI float xrow_sum(float x) {
  auto a = __builtin_amdgcn_permlane16_swap(asu(x), asu(x), false, false);
  x = asf(a[0]) + asf(a[1]);
  auto b = __builtin_amdgcn_permlane32_swap(asu(x), asu(x), false, false);
  return asf(b[0]) + asf(b[1]);
}
DI float xrow_max(float x) {
  auto a = __builtin_amdgcn_permlane16_swap(asu(x), asu(x), false, false);
  x = fmaxf(asf(a[0]), asf(a[1]));
  auto b = __builtin_amdgcn_permlane32_swap(asu(x), asu(x), false, false);
  return fmaxf(asf(b[0]), asf(b[1]));
}
DI float ex2(float x) { return __builtin_amdgcn_exp2f(x); }
template <typename T> DI T ntload(const void* p) { return __builtin_nontemporal_load(reinterpret_cast<const T*>(p)); }
#define MFMA32(a, b, c) __builtin_amdgcn_mfma_f32_32x32x16_bf16(__builtin_bit_cast(bf16x8, a), __builtin_bit_cast(bf16x8, b), (c), 0, 0, 0)
DI u32x4 pack8(const f32x16& x, int s) {
  u32x4 p;
#pragma unroll
  for (int j = 0; j < 4; ++j) p[j] = pack2(x[8 * s + 2 * j], x[8 * s + 2 * j + 1]);
  return p;
}
DI int opaque(int x) { asm volatile("" : "+v"(x)); return x; }
#define VM0() asm volatile("s_waitcnt vmcnt(0)" ::: "memory")
#define CBAR() asm volatile("" ::: "memory")
#define WGBAR() do { asm volatile("s_waitcnt lgkmcnt(0)" ::: "memory"); __builtin_amdgcn_s_barrier(); asm volatile("" ::: "memory"); } while (0)

#define STAMP(k) do { } while (0)

typedef __amdgpu_buffer_rsrc_t rsrc_t;
struct Tag { unsigned lo, hi; };
DI Tag load_tag(unsigned char* ws, size_t off) {
  const u64 g = __hip_atomic_load((GAS u64*)(ws + off), RLX_AGENT) + 1ull;
  Tag t;
  t.lo = __builtin_amdgcn_readfirstlane((unsigned)g);
  t.hi = __builtin_amdgcn_readfirstlane((unsigned)(g >> 32));
  return t;
}
DI void st_g(rsrc_t rs, int idx, float v0, float v1, Tag t) {
  const u32x4 x = {asu(v0), asu(v1), t.lo, t.hi};
  __builtin_amdgcn_raw_buffer_store_b128(x, rs, (int)Z_GRAN + idx * 16, 0, 16);
}
DI u32x4 ld_g(rsrc_t rs, int idx) { return __builtin_amdgcn_raw_buffer_load_b128(rs, (int)Z_GRAN + idx * 16, 0, 16); }
DI bool tag_ok(const u32x4& x, Tag t) { return (x[2] == t.lo) & (x[3] == t.hi); }
DI void st_flag(rsrc_t rs, int off, Tag t) {
  const u32x2 x = {t.lo, t.hi};
  __builtin_amdgcn_raw_buffer_store_b64(x, rs, off, 0, 16);
}
DI bool spin_fail(unsigned& spins) {
  __builtin_amdgcn_s_sleep(1);
  return ++spins > kSpinCap;
}
DI void prepoll(rsrc_t rs, int idx, int stride, int n, Tag t, int lane) {
  for (unsigned spins = 0;;) {
    bool ok = true;
    if (lane < n) ok = tag_ok(ld_g(rs, idx + lane * stride), t);
    if (__all(ok)) break;
    __builtin_amdgcn_s_sleep(POLL_SLEEP);
    if (++spins > kSpinCap) break;
  }
  CBAR();
}
template <int N> DI void gather(rsrc_t rs, int idx, int stride, Tag t, float (&v)[N]) {
  for (unsigned spins = 0;;) {
    bool ok = true;
#pragma unroll
    for (int k = 0; k < N; ++k) {
      const u32x4 x = ld_g(rs, idx + k * stride);
      v[k] = asf(x[0]);
      ok &= tag_ok(x, t);
    }
    if (__all(ok)) break;
    if (spin_fail(spins)) break;
  }
  CBAR();
}

DI void fma_row(float (&acc)[12], float p, const u32x4& a, const u32x2& b) {
#pragma unroll
  for (int k = 0; k < 4; ++k) { acc[2 * k] = fmaf(p, bflo(a[k]), acc[2 * k]); acc[2 * k + 1] = fmaf(p, bfhi(a[k]), acc[2 * k + 1]); }
#pragma unroll
  for (int k = 0; k < 2; ++k) { acc[8 + 2 * k] = fmaf(p, bflo(b[k]), acc[8 + 2 * k]); acc[8 + 2 * k + 1] = fmaf(p, bfhi(b[k]), acc[8 + 2 * k + 1]); }
}
DI void rows_to_lds(float (&acc)[12], LAS float* dst, int lane) {
  const int r = lane >> 4, c = lane & 15;
#pragma unroll
  for (int k = 0; k < 12; ++k) acc[k] = xrow_sum(acc[k]);
  if (r == 0) {
#pragma unroll
    for (int k = 0; k < 8; ++k) dst[8 * c + k] = acc[k];
#pragma unroll
    for (int k = 0; k < 4; ++k) dst[128 + 4 * c + k] = acc[8 + k];
  }
}

DI void out_reduce(rsrc_t rs, unsigned char* ws, u16* __restrict__ out, LAS unsigned char* L, int mat, int col0, int tid, Tag t) {
  const int col = tid & 127, qd = tid >> 7;
  float v[8];
  gather<8>(rs, I_OUT + (mat * NH + qd * 8) * DM + col0 + col, DM, t, v);
  LAS float* red = (LAS float*)(L + L_RED);
  red[tid] = ((v[0] + v[1]) + (v[2] + v[3])) + ((v[4] + v[5]) + (v[6] + v[7]));
  WGBAR();
  if (tid < 128) out[mat * DM + col0 + tid] = f2bf((red[tid] + red[128 + tid]) + (red[256 + tid] + red[384 + tid]));
  if (col0 == 0 && tid == 0) st_flag(rs, (int)(mat ? Z_GENB : Z_GENA), t);
}

constexpr int NQ = 5;
struct QLoads { u32x4 a[8]; u32x2 b[8]; float x[8]; };
DI void q_issue(QLoads& Q, const u16* __restrict__ xA, const u16* __restrict__ wqA, const u16* __restrict__ xB,
                const u16* __restrict__ wqB, int h, int jj, int wave, int lane) {
  const int r = lane >> 4, c = lane & 15;
  const u16* x = wave < 4 ? xA : xB;
  const u16* w = wave < 4 ? wqA : wqB;
#pragma unroll
  for (int i = 0; i < 8; ++i) {
    const int row = 128 * jj + 32 * (wave & 3) + 4 * i + r;
    const u16* p = w + (size_t)row * HD + h * DH;
    Q.a[i] = ntload<u32x4>(p + 8 * c); Q.b[i] = ntload<u32x2>(p + 128 + 4 * c);
    Q.x[i] = bflo((unsigned)x[row]);
  }
}
DI void q_partial(QLoads& Q, LAS unsigned char* L, int wave, int lane) {
  float acc[12];
#pragma unroll
  for (int k = 0; k < 12; ++k) acc[k] = 0.f;
#pragma unroll
  for (int i = 0; i < 8; ++i) fma_row(acc, Q.x[i], Q.a[i], Q.b[i]);
  rows_to_lds(acc, (LAS float*)(L + L_RED) + wave * DH, lane);
}
DI void q_publish(rsrc_t rs, LAS unsigned char* L, int h, int jj, int wave, int lane, Tag ta, Tag tb) {
  const int tid = wave * 64 + lane;
  const LAS float* red = (const LAS float*)(L + L_RED);
  if (wave < 6) {
    const int mt = wave >= 3 ? 1 : 0, d = tid - mt * DH;
    const LAS float* rp = red + mt * 4 * DH + d;
    st_g(rs, I_QP + (h * NQ + jj) * 384 + tid, (rp[0] + rp[DH]) + (rp[2 * DH] + rp[3 * DH]), 0.f, mt ? tb : ta);
  }
}
DI void q_gather(rsrc_t rs, LAS unsigned char* L, int h, int part, int wave, int lane, Tag t) {
  const int tid = wave * 64 + lane;
  if (wave < 3) {
    float v[NQ];
    prepoll(rs, I_QP + h * NQ * 384 + part * DH + wave * 64 + 63, 384, NQ, t, lane);
    gather<NQ>(rs, I_QP + h * NQ * 384 + part * DH + tid, 384, t, v);
    ((LAS u16*)(L + L_QS))[tid] = f2bf(((v[0] + v[1]) + (v[2] + v[3])) + v[4]);
  }
}

DI void a_role(const u16* __restrict__ xA, const u16* __restrict__ wqA, const u16* __restrict__ xB, const u16* __restrict__ wqB,
               const u16* __restrict__ K, const u16* __restrict__ V, const u16* __restrict__ woA,
               unsigned char* ws, u16* __restrict__ out, LAS unsigned char* L, int h, int j, int wave, int lane) {
  const int r = lane >> 4, c = lane & 15, tid = wave * 64 + lane;
  STAMP(0);
  const rsrc_t rs = __builtin_amdgcn_make_buffer_rsrc((void*)ws, 0, (int)W_END, 0x00020000);
  const Tag ta = load_tag(ws, Z_GENA), tb = load_tag(ws, Z_GENB);
  QLoads Q;
  if (j < NQ) q_issue(Q, xA, wqA, xB, wqB, h, j, wave, lane);
  constexpr int KR = A_KR, TK = 4 * KR, NTH = SK / TK;
  constexpr int NST = A_NST, DYN0 = 48 * NST;
  static_assert(NST >= 2 && DYN0 <= NTH, "tile split");
  const int gw = j * 8 + wave;
  const int t_beg = j * 8 * NST + wave;
  const size_t base = ((size_t)h * SK + r) * DH;
  const u16* kp = K + base;
  const u16* vp = V + base;
  u32x4 ka[KR], va[KR]; u32x2 kb[KR], vb[KR];
#define LOADT(t) do { _Pragma("unroll") for (int i = 0; i < KR; ++i) { \
    const u16* kr = kp + (size_t)((t) * TK + i * 4) * DH; const u16* vr = vp + (size_t)((t) * TK + i * 4) * DH; \
    ka[i] = ntload<u32x4>(kr + 8 * c); kb[i] = ntload<u32x2>(kr + 128 + 4 * c); \
    va[i] = ntload<u32x4>(vr + 8 * c); vb[i] = ntload<u32x2>(vr + 128 + 4 * c); } } while (0)
  GAS unsigned* tcnt = (GAS unsigned*)(ws + Z_TCNT) + h * 32;
  if (j == 0 && tid == 0) __hip_atomic_store(tcnt, 0u, RLX_AGENT);
  if (j < NQ) q_partial(Q, L, wave, lane);
  if (j == 0 && wave == 0) VM0();
  STAMP(10);
  WGBAR();
  STAMP(11);
  __builtin_amdgcn_sched_barrier(0);
  if (wave >= EARLY_W) LOADT(t_beg);
  __builtin_amdgcn_sched_barrier(0);
  if (j < NQ) q_publish(rs, L, h, j, wave, lane, ta, tb);
  q_gather(rs, L, h, 0, wave, lane, ta);
  STAMP(12);
  __builtin_amdgcn_sched_barrier(0);
  if (wave < EARLY_W) LOADT(t_beg);
  __builtin_amdgcn_sched_barrier(0);
  WGBAR();
  STAMP(1);
  const u32x4 qa = *(const LAS u32x4*)(L + L_QS + 16 * c);
  const u32x2 qb = *(const LAS u32x2*)(L + L_QS + 256 + 8 * c);
  float m = -INFINITY, l = 0.f, acc[12];
#pragma unroll
  for (int k = 0; k < 12; ++k) acc[k] = 0.f;
  unsigned nx = 0u;
  for (int i = 0;; ++i) {
    const int t_next = (i + 1 < NST) ? t_beg + 8 * (i + 1) : DYN0 + __builtin_amdgcn_readfirstlane((int)nx);
    if (i + 2 >= NST && t_next < NTH && lane == 0) nx = __hip_atomic_fetch_add(tcnt, 1u, RLX_AGENT);
    __builtin_amdgcn_sched_barrier(0);
    {
      float s[KR];
      float mn = m;
#pragma unroll
      for (int q = 0; q < KR; ++q) {
        float d = 0.f;
#pragma unroll
        for (int k = 0; k < 4; ++k) d = dot2(ka[q][k], qa[k], d);
        d = dot2(kb[q][0], qb[0], d); d = dot2(kb[q][1], qb[1], d);
        d = row_sum16(d);
        s[q] = rbf(rbf(d) * SCALE);
        mn = fmaxf(mn, s[q]);
      }
      const float alpha = ex2((m - mn) * LOG2E);
      m = mn;
      float ps = 0.f;
#pragma unroll
      for (int q = 0; q < KR; ++q) { s[q] = ex2((s[q] - mn) * LOG2E); ps += s[q]; }
      l = l * alpha + ps;
#pragma unroll
      for (int k = 0; k < 12; ++k) acc[k] *= alpha;
#pragma unroll
      for (int q = 0; q < KR; ++q) fma_row(acc, s[q], va[q], vb[q]);
    }
    if (t_next >= NTH) break;
    LOADT(t_next);
  }
#undef LOADT
  STAMP(2);
  u32x4 wv[6];
  if (j < 5) {
#pragma unroll
    for (int i = 0; i < 6; ++i) wv[i] = ntload<u32x4>(woA + (size_t)(h * DH + 24 * wave + 4 * i + r) * DM + 128 * j + 8 * c);
  }
  const float M = xrow_max(m);
  const float f = ex2((m - M) * LOG2E);
  l = xrow_sum(l * f);
#pragma unroll
  for (int k = 0; k < 12; ++k) acc[k] *= f;
  LAS float* red = (LAS float*)(L + L_RED);
  rows_to_lds(acc, red + wave * 200 + 8, lane);
  if (lane == 0) { red[wave * 200] = M; red[wave * 200 + 1] = l; }
  STAMP(6);
  WGBAR();
  STAMP(7);
  {
    float mw[8], MM = -INFINITY;
#pragma unroll
    for (int w = 0; w < 8; ++w) { mw[w] = red[w * 200]; MM = fmaxf(MM, mw[w]); }
    const int idx = tid < DH ? 8 + tid : 1;
    float s = 0.f;
#pragma unroll
    for (int w = 0; w < 8; ++w) s = fmaf(ex2((mw[w] - MM) * LOG2E), red[w * 200 + idx], s);
    const int ga = I_AP + (h * NA + j) * 200;
    if (tid < DH) st_g(rs, ga + 8 + tid, s, 0.f, ta);
    if (tid == DH) st_g(rs, ga, MM, s, ta);
  }
  STAMP(8);
  if (j < 5) {
    if (wave < 3) {
      float v[NA], mm[NA], ll[NA];
      const int gh = I_AP + h * NA * 200;
      for (unsigned spins = 0;;) {
        bool ok = true;
#pragma unroll
        for (int k = 0; k < NA; ++k) {
          const u32x4 x = ld_g(rs, gh + k * 200 + 8 + tid), y = ld_g(rs, gh + k * 200);
          v[k] = asf(x[0]); mm[k] = asf(y[0]); ll[k] = asf(y[1]);
          ok &= tag_ok(x, ta) & tag_ok(y, ta);
        }
        if (__all(ok)) break;
        if (spin_fail(spins)) break;
      }
      CBAR();
      float Mx = mm[0];
#pragma unroll
      for (int k = 1; k < NA; ++k) Mx = fmaxf(Mx, mm[k]);
      float Lsum = 0.f, o = 0.f;
#pragma unroll
      for (int k = 0; k < NA; ++k) { const float g = ex2((mm[k] - Mx) * LOG2E); Lsum = fmaf(g, ll[k], Lsum); o = fmaf(g, v[k], o); }
      ((LAS float*)(L + L_VEC))[tid] = rbf(o / Lsum);
    }
    WGBAR();
    STAMP(3);
    const LAS float* vec = (const LAS float*)(L + L_VEC);
    float a8[8];
#pragma unroll
    for (int k = 0; k < 8; ++k) a8[k] = 0.f;
#pragma unroll
    for (int i = 0; i < 6; ++i) {
      const float x = vec[24 * wave + 4 * i + r];
#pragma unroll
      for (int k = 0; k < 4; ++k) { a8[2 * k] = fmaf(x, bflo(wv[i][k]), a8[2 * k]); a8[2 * k + 1] = fmaf(x, bfhi(wv[i][k]), a8[2 * k + 1]); }
    }
#pragma unroll
    for (int k = 0; k < 8; ++k) a8[k] = xrow_sum(a8[k]);
    LAS float* r2 = (LAS float*)(L + L_BIG);
    if (r == 0) {
#pragma unroll
      for (int k = 0; k < 8; ++k) r2[wave * 128 + 8 * c + k] = a8[k];
    }
    WGBAR();
    if (tid < 128) {
      float s = 0.f;
#pragma unroll
      for (int w = 0; w < 8; ++w) s += r2[w * 128 + tid];
      st_g(rs, I_OUT + h * DM + 128 * j + tid, s, 0.f, ta);
    }
    STAMP(4);
    if (h == 4 * j + 1) out_reduce(rs, ws, out, L, 0, 128 * j, tid, ta);
    STAMP(5);
  }
}

DI void b_role(const u16* __restrict__ xA, const u16* __restrict__ wqA, const u16* __restrict__ xB, const u16* __restrict__ wqB,
               const u16* __restrict__ ckv, const u16* __restrict__ wuk, const u16* __restrict__ wuv, const u16* __restrict__ woB,
               unsigned char* ws, u16* __restrict__ out, LAS unsigned char* L, int h, int half, int wave, int lane) {
  const int r = lane >> 4, c = lane & 15, tid = wave * 64 + lane;
  const int cl = lane & 31, hh = lane >> 5;
  const int kb_ = 2 * h + half;
  const rsrc_t rs = __builtin_amdgcn_make_buffer_rsrc((void*)ws, 0, (int)W_END, 0x00020000);
  STAMP(0);
  const Tag tb = load_tag(ws, Z_GENB);
  u32x4 cf[4][6];
  {
    u32x4 ua[12]; u32x2 ub[12];
#define LOADU() do { _Pragma("unroll") for (int i = 0; i < 12; ++i) { \
      const u16* row = wuk + ((size_t)(384 * half + 48 * wave + 4 * i + r) * NH + h) * DH; \
      ua[i] = ntload<u32x4>(row + 8 * c); ub[i] = ntload<u32x2>(row + 128 + 4 * c); } } while (0)
#define LOADC() do { _Pragma("unroll") for (int t = 0; t < 4; ++t) _Pragma("unroll") for (int i = 0; i < 6; ++i) \
        cf[t][i] = ntload<u32x4>(ckv + (size_t)(128 * kb_ + 32 * t) * DC + (size_t)(384 * wave + 64 * i + lane) * 8); } while (0)
    LOADU();
    __builtin_amdgcn_sched_barrier(0);
    if (wave >= 3) LOADC();
    __builtin_amdgcn_sched_barrier(0);
    q_gather(rs, L, h, 1, wave, lane, tb);
    STAMP(15);
    __builtin_amdgcn_sched_barrier(0);
    if (wave == 2) LOADC();
    __builtin_amdgcn_sched_barrier(0);
    WGBAR();
    STAMP(1);
    const u32x4 qa = *(const LAS u32x4*)(L + L_QS + 16 * c);
    const u32x2 qb = *(const LAS u32x2*)(L + L_QS + 256 + 8 * c);
    LAS float* qv = (LAS float*)(L + L_VEC);
#pragma unroll
    for (int i = 0; i < 12; ++i) {
      float s = 0.f;
#pragma unroll
      for (int k = 0; k < 4; ++k) s = dot2(ua[i][k], qa[k], s);
      s = dot2(ub[i][0], qb[0], s); s = dot2(ub[i][1], qb[1], s);
      s = row_sum16(s);
      if (c == 0) qv[48 * wave + 4 * i + r] = s;
    }
    WGBAR();
    if (wave < 2) {
      if (lane < 48) {
        u32x4 o;
#pragma unroll
        for (int k = 0; k < 4; ++k) {
          const float a = qv[8 * lane + 2 * k], b = qv[8 * lane + 2 * k + 1];
          const float ah = rbf(a), bh = rbf(b);
          o[k] = wave ? pack2(a - ah, b - bh) : pack2(ah, bh);
        }
        const int c8 = 48 * half + lane;
        __builtin_amdgcn_raw_buffer_store_b128(o, rs, (int)(wave ? W_QL : W_QH) + ((c8 >> 1) * 64 + h + 32 * (c8 & 1)) * 16, 0, 16);
      }
      VM0();
      if (lane == 0) st_flag(rs, (int)Z_FQABS + (2 * kb_ + wave) * 8, tb);
      __builtin_amdgcn_sched_barrier(0);
      LOADC();
    }
#undef LOADC
#undef LOADU
  }
  STAMP(2);
#pragma unroll
  for (int rd = 0; rd < 2; ++rd) {
#pragma unroll
    for (int tt = 0; tt < 2; ++tt)
#pragma unroll
      for (int i = 0; i < 6; ++i) {
        const int e = 384 * wave + 64 * i + lane;
        *(LAS u32x4*)(L + L_BIG + tt * 49664 + (e / 96) * 1552 + (e % 96) * 16) = cf[2 * rd + tt][i];
      }
    WGBAR();
#pragma unroll
    for (int tt = 0; tt < 2; ++tt)
#pragma unroll
      for (int s = 0; s < 6; ++s)
        cf[2 * rd + tt][s] = *(const LAS u32x4*)(L + L_BIG + tt * 49664 + cl * 1552 + (96 * wave + 16 * s + 8 * hh) * 2);
    WGBAR();
  }
  {
    if (wave == 0) {
      for (unsigned spins = 0;;) {
        const u32x4 f = __builtin_amdgcn_raw_buffer_load_b128(rs, (int)Z_FQABS + lane * 16, 0, 16);
        if (__all((f[0] == tb.lo) & (f[1] == tb.hi) & (f[2] == tb.lo) & (f[3] == tb.hi))) break;
        __builtin_amdgcn_s_sleep(POLL_SLEEP);
        if (++spins > kSpinCap) break;
      }
    }
    WGBAR();
    STAMP(3);
    u32x4 qh[6], ql[6];
#pragma unroll
    for (int s = 0; s < 6; ++s) {
      qh[s] = __builtin_amdgcn_raw_buffer_load_b128(rs, (int)W_QH + ((wave * 6 + s) * 64 + lane) * 16, 0, 16);
      ql[s] = __builtin_amdgcn_raw_buffer_load_b128(rs, (int)W_QL + ((wave * 6 + s) * 64 + lane) * 16, 0, 16);
    }
    __builtin_amdgcn_sched_barrier(0);
    f32x16 zero;
#pragma unroll
    for (int i = 0; i < 16; ++i) zero[i] = 0.f;
    LAS f32x4* sp = (LAS f32x4*)(L + L_BIG);
#pragma unroll
    for (int t = 0; t < 4; ++t) {
      f32x16 S = zero;
#pragma unroll
      for (int s = 0; s < 6; ++s) S = MFMA32(cf[t][s], ql[s], S);
#pragma unroll
      for (int s = 0; s < 6; ++s) S = MFMA32(cf[t][s], qh[s], S);
#pragma unroll
      for (int g = 0; g < 4; ++g) sp[((wave * 4 + t) * 4 + g) * 64 + lane] = f32x4{S[4 * g], S[4 * g + 1], S[4 * g + 2], S[4 * g + 3]};
    }
    STAMP(4);
    WGBAR();
    float sv[8];
    {
      const int t = wave >> 1;
#pragma unroll
      for (int gg = 0; gg < 2; ++gg) {
        const int g = 2 * (wave & 1) + gg;
        f32x4 a = sp[((0 * 4 + t) * 4 + g) * 64 + lane];
#pragma unroll
        for (int w = 1; w < 8; ++w) a += sp[((w * 4 + t) * 4 + g) * 64 + lane];
#pragma unroll
        for (int k = 0; k < 4; ++k) sv[4 * gg + k] = rbf(rbf(a[k]) * SCALE);
      }
    }
    float m = fmaxf(fmaxf(fmaxf(sv[0], sv[1]), fmaxf(sv[2], sv[3])), fmaxf(fmaxf(sv[4], sv[5]), fmaxf(sv[6], sv[7])));
    m = fmaxf(m, __shfl_xor(m, 32));
    LAS float* mx = (LAS float*)(L + L_RED);
    if (lane < 32) mx[wave * 32 + lane] = m;
    WGBAR();
#pragma unroll
    for (int w = 0; w < 8; ++w) m = fmaxf(m, mx[w * 32 + cl]);
    float l = 0.f;
#pragma unroll
    for (int k = 0; k < 8; ++k) { sv[k] = ex2((sv[k] - m) * LOG2E); l += sv[k]; }
    l += __shfl_xor(l, 32);
    if (lane < 32) mx[256 + wave * 32 + lane] = l;
    LAS u32x4* pf = (LAS u32x4*)(L + L_SF);
    pf[wave * 64 + lane] = u32x4{pack2(sv[0], sv[1]), pack2(sv[2], sv[3]), pack2(sv[4], sv[5]), pack2(sv[6], sv[7])};
    WGBAR();
    u32x4 pb[4][2];
#pragma unroll
    for (int t = 0; t < 4; ++t) { pb[t][0] = pf[(2 * t) * 64 + lane]; pb[t][1] = pf[(2 * t + 1) * 64 + lane]; }
    if (wave == 0 && lane < 32) {
      float ls = 0.f;
#pragma unroll
      for (int w = 0; w < 8; ++w) ls += mx[256 + w * 32 + lane];
      const u32x2 ml = {asu(m), asu(ls)};
      __builtin_amdgcn_raw_buffer_store_b64(ml, rs, (int)W_BML + ((cl * NSB + kb_) * 2) * 4, 0, 16);
    }
    u32x4 idlo, idhi;
#pragma unroll
    for (int k = 0; k < 4; ++k) {
      const int k0 = 8 * hh + 2 * k, k1 = k0 + 1;
      idlo[k] = (cl == k0 ? 0x3f80u : 0u) | (cl == k1 ? 0x3f800000u : 0u);
      idhi[k] = (cl == k0 + 16 ? 0x3f80u : 0u) | (cl == k1 + 16 ? 0x3f800000u : 0u);
    }
    STAMP(5);
    LAS float* ost = (LAS float*)(L + L_BIG) + wave * 3200;
#pragma unroll
    for (int ct = 0; ct < 3; ++ct) {
      f32x16 O = zero;
#pragma unroll
      for (int t = 0; t < 4; ++t) {
        f32x16 T = MFMA32(cf[t][2 * ct], idlo, zero);
        T = MFMA32(cf[t][2 * ct + 1], idhi, T);
        O = MFMA32(pack8(T, 0), pb[t][0], O);
        O = MFMA32(pack8(T, 1), pb[t][1], O);
      }
#pragma unroll
      for (int g = 0; g < 4; ++g)
        *(LAS f32x4*)(ost + cl * 100 + 32 * ct + 8 * g + 4 * hh) = f32x4{O[4 * g], O[4 * g + 1], O[4 * g + 2], O[4 * g + 3]};
    }
    asm volatile("s_waitcnt lgkmcnt(0)" ::: "memory");
#pragma unroll
    for (int i = 0; i < 6; ++i) {
      const int e = i * 64 + lane, hd = e / 12, q8 = e % 12;
      const f32x4 v0 = *(const LAS f32x4*)(ost + hd * 100 + 8 * q8), v1 = *(const LAS f32x4*)(ost + hd * 100 + 8 * q8 + 4);
      const u32x4 o = {pack2(v0[0], v0[1]), pack2(v0[2], v0[3]), pack2(v1[0], v1[1]), pack2(v1[2], v1[3])};
      __builtin_amdgcn_raw_buffer_store_b128(o, rs, (int)W_BO + ((kb_ * NH + hd) * DC + 96 * wave + 8 * q8) * 2, 0, 16);
    }
    STAMP(6);
    VM0();
    WGBAR();
    if (tid == 0) st_flag(rs, (int)Z_FBATT + kb_ * 8, tb);
    STAMP(7);
  }
  {
    const int lane2 = opaque(lane);
    const int r = lane2 >> 4, c = lane2 & 15, tid = wave * 64 + lane2;
    u32x4 va[12]; u32x2 vb[12];
#pragma unroll
    for (int i = 0; i < 12; ++i) {
      const u16* row = wuv + ((size_t)(384 * half + 48 * wave + 4 * i + r) * NH + h) * DH;
      va[i] = ntload<u32x4>(row + 8 * c); vb[i] = ntload<u32x2>(row + 128 + 4 * c);
    }
    __builtin_amdgcn_sched_barrier(0);
    if (wave == 0) {
      for (unsigned spins = 0;;) {
        const u32x2 f = __builtin_amdgcn_raw_buffer_load_b64(rs, (int)Z_FBATT + lane2 * 8, 0, 16);
        if (__all((f[0] == tb.lo) & (f[1] == tb.hi))) break;
        __builtin_amdgcn_s_sleep(POLL_SLEEP);
        if (++spins > kSpinCap) break;
      }
    }
    WGBAR();
    STAMP(8);
    const u32x2 st = __builtin_amdgcn_raw_buffer_load_b64(rs, (int)W_BML + (h * NSB + lane2) * 8, 0, 16);
    u32x4 pa[8];
    const int l48 = lane2 < 48 ? lane2 : 0;
#pragma unroll
    for (int i = 0; i < 8; ++i)
      pa[i] = __builtin_amdgcn_raw_buffer_load_b128(rs, (int)W_BO + (((8 * wave + i) * NH + h) * DC + 384 * half + 8 * l48) * 2, 0, 16);
    const float m0 = asf(st[0]), l0 = asf(st[1]);
    float M = m0;
#pragma unroll
    for (int o = 1; o < 64; o <<= 1) M = fmaxf(M, __shfl_xor(M, o));
    const float f0 = ex2((m0 - M) * LOG2E);
    float Ls = f0 * l0;
#pragma unroll
    for (int o = 1; o < 64; o <<= 1) Ls += __shfl_xor(Ls, o);
    const float w0 = f0 / Ls;
    float sa[8];
#pragma unroll
    for (int k = 0; k < 8; ++k) sa[k] = 0.f;
#pragma unroll
    for (int i = 0; i < 8; ++i) {
      const float w = __shfl(w0, 8 * wave + i);
#pragma unroll
      for (int k = 0; k < 4; ++k) { sa[2 * k] = fmaf(w, bflo(pa[i][k]), sa[2 * k]); sa[2 * k + 1] = fmaf(w, bfhi(pa[i][k]), sa[2 * k + 1]); }
    }
    LAS f32x4* acc4 = (LAS f32x4*)(L + L_RED);
    if (lane2 < 48) {
      acc4[wave * 96 + 2 * lane2] = f32x4{sa[0], sa[1], sa[2], sa[3]};
      acc4[wave * 96 + 2 * lane2 + 1] = f32x4{sa[4], sa[5], sa[6], sa[7]};
    }
    __builtin_amdgcn_sched_barrier(0);
    u32x4 wo[24];
    const int lc = lane2 < 40 ? lane2 : 0;
#pragma unroll
    for (int i = 0; i < 24; ++i) wo[i] = ntload<u32x4>(woB + (size_t)(h * DH + 24 * wave + i) * DM + 320 * half + 8 * lc);
    __builtin_amdgcn_sched_barrier(0);
    WGBAR();
    if (tid < 384) {
      const LAS float* af = (const LAS float*)(L + L_RED) + tid;
      float s = 0.f;
#pragma unroll
      for (int w = 0; w < 8; ++w) s += af[w * 384];
      ((LAS float*)(L + L_VEC))[tid] = s;
    }
    WGBAR();
    STAMP(9);
    float acc[12];
#pragma unroll
    for (int k = 0; k < 12; ++k) acc[k] = 0.f;
    const LAS float* cv = (const LAS float*)(L + L_VEC);
#pragma unroll
    for (int i = 0; i < 12; ++i) fma_row(acc, cv[48 * wave + 4 * i + r], va[i], vb[i]);
    LAS float* red = (LAS float*)(L + L_RED);
    rows_to_lds(acc, red + wave * DH, lane2);
    WGBAR();
    if (tid < DH) {
      float s = 0.f;
#pragma unroll
      for (int w = 0; w < 8; ++w) s += red[w * DH + tid];
      st_g(rs, I_CTX + (h * 2 + half) * DH + tid, s, 0.f, tb);
      float v[1];
      gather<1>(rs, I_CTX + (h * 2 + (half ^ 1)) * DH + tid, 0, tb, v);
      ((LAS float*)(L + L_VEC))[tid] = rbf(half ? v[0] + s : s + v[0]);
    }
    WGBAR();
    STAMP(10);
    float a8[8];
#pragma unroll
    for (int k = 0; k < 8; ++k) a8[k] = 0.f;
#pragma unroll
    for (int i = 0; i < 24; ++i) {
      const float x = cv[24 * wave + i];
#pragma unroll
      for (int k = 0; k < 4; ++k) { a8[2 * k] = fmaf(x, bflo(wo[i][k]), a8[2 * k]); a8[2 * k + 1] = fmaf(x, bfhi(wo[i][k]), a8[2 * k + 1]); }
    }
    LAS float* r2 = (LAS float*)(L + L_BIG);
    if (lane2 < 40) {
#pragma unroll
      for (int k = 0; k < 8; ++k) r2[wave * 320 + 8 * lane2 + k] = a8[k];
    }
    WGBAR();
    if (tid < 320) {
      float s = 0.f;
#pragma unroll
      for (int w = 0; w < 8; ++w) s += r2[w * 320 + tid];
      st_g(rs, I_OUT + (NH + h) * DM + 320 * half + tid, s, 0.f, tb);
    }
    STAMP(11);
    if ((kb_ % 12) == 3 && kb_ < 60) out_reduce(rs, ws, out, L, 1, 128 * (kb_ / 12), tid, tb);
    STAMP(12);
  }
}

__global__ void __launch_bounds__(512, 2) mega(const u16* __restrict__ xA, const u16* __restrict__ K, const u16* __restrict__ V,
                                               const u16* __restrict__ wqA, const u16* __restrict__ woA,
                                               const u16* __restrict__ xB, const u16* __restrict__ ckv, const u16* __restrict__ wqB,
                                               const u16* __restrict__ wuk, const u16* __restrict__ wuv, const u16* __restrict__ woB,
                                               unsigned char* ws, u16* __restrict__ out) {
  extern __shared__ __attribute__((aligned(16))) unsigned char smem[];
  LAS unsigned char* L = (LAS unsigned char*)smem;
  const int tid = threadIdx.x, lane = tid & 63;
  const int wave = __builtin_amdgcn_readfirstlane(tid >> 6);
  const int b = blockIdx.x;
  const int h = b >> 3, jj = (b + (b >> 3)) & 7;
  if (jj < NA) a_role(xA, wqA, xB, wqB, K, V, woA, ws, out, L, h, jj, wave, lane);
  else b_role(xA, wqA, xB, wqB, ckv, wuk, wuv, woB, ws, out, L, h, jj - NA, wave, lane);
}

extern "C" void kernel_launch(void* const* d_in, const int* in_sizes, int n_in,
                              void* d_out, int out_size, void* d_ws, size_t ws_size,
                              hipStream_t stream) {
  (void)hipFuncSetAttribute((const void*)mega, hipFuncAttributeMaxDynamicSharedMemorySize, L_BYTES);
  mega<<<256, 512, L_BYTES, stream>>>((const u16*)d_in[0], (const u16*)d_in[1], (const u16*)d_in[2], (const u16*)d_in[3], (const u16*)d_in[4],
                                      (const u16*)d_in[5], (const u16*)d_in[6], (const u16*)d_in[7], (const u16*)d_in[8], (const u16*)d_in[9],
                                      (const u16*)d_in[10], (unsigned char*)d_ws, (u16*)d_out);
}
```

```cpp
#include <hip/hip_runtime.h>
#include <hip/hip_bf16.h>
#include <stdint.h>

typedef unsigned u32x4 __attribute__((ext_vector_type(4)));
typedef unsigned u32x2 __attribute__((ext_vector_type(2)));
typedef __bf16 bf16x2 __attribute__((ext_vector_type(2)));
typedef __bf16 bf16x8 __attribute__((ext_vector_type(8)));
typedef float f32x16 __attribute__((ext_vector_type(16)));
typedef float f32x4 __attribute__((ext_vector_type(4)));
typedef unsigned short u16;
typedef unsigned long long u64;

#define DI __device__ __forceinline__
#define GAS __attribute__((address_space(1)))
#define LAS __attribute__((address_space(3)))
#define RLX_AGENT __ATOMIC_RELAXED, __HIP_MEMORY_SCOPE_AGENT

constexpr int NH = 32, SK = 8192, DH = 192, DM = 640, DC = 768, HD = NH * DH;
constexpr float SCALE = 0.07216878364870322f;
constexpr float LOG2E = 1.4426950408889634f;
#define EARLY_W 3
#define POLL_SLEEP 4
constexpr int NA = 6;
constexpr int NSB = 64;
constexpr unsigned kSpinCap = 1u << 18;

constexpr size_t Z_GENA = 0, Z_GENB = 128;
constexpr size_t Z_FQABS = 1024;
constexpr size_t Z_FBATT = 2048;
constexpr size_t Z_GRAN = 4096;
constexpr int I_QP = 0;
constexpr int I_CTX = I_QP + NH * 5 * 384;
constexpr int I_AP = I_CTX + NH * 2 * 192;
constexpr int I_OUT = I_AP + NH * NA * 200;
constexpr int I_END = I_OUT + 2 * NH * DM;
constexpr size_t W_QH = Z_GRAN + (size_t)I_END * 16;
constexpr size_t W_QL = W_QH + (size_t)NH * DC * 2;
constexpr size_t W_BML = W_QL + (size_t)NH * DC * 2;
constexpr size_t W_BO = W_BML + (size_t)NH * NSB * 2 * 4;
constexpr size_t W_END = W_BO + (size_t)NSB * NH * DC * 2;
static_assert(W_END < (1ull << 31), "ws layout");

constexpr int L_QS = 0;
constexpr int L_VEC = 512;
constexpr int L_RED = 2048;
constexpr int L_BIG = 16384;
constexpr int L_SF = L_BIG + 131072;
constexpr int L_BYTES = L_SF + 16384;

DI float dot2(unsigned a, unsigned b, float c) {
  return __builtin_amdgcn_fdot2_f32_bf16(__builtin_bit_cast(bf16x2, a), __builtin_bit_cast(bf16x2, b), c, false);
}
DI float rbf(float x) { return (float)(__bf16)x; }
DI float asf(unsigned v) { return __builtin_bit_cast(float, v); }
DI unsigned asu(float v) { return __builtin_bit_cast(unsigned, v); }
DI float bflo(unsigned v) { return __builtin_bit_cast(float, v << 16); }
DI float bfhi(unsigned v) { return __builtin_bit_cast(float, v & 0xffff0000u); }
DI u16 f2bf(float x) { return __builtin_bit_cast(u16, (__bf16)x); }
DI unsigned pack2(float lo, float hi) { return (unsigned)f2bf(lo) | ((unsigned)f2bf(hi) << 16); }
template <int CTRL> DI float dpp(float x) {
  return __builtin_bit_cast(float, __builtin_amdgcn_mov_dpp(__builtin_bit_cast(int, x), CTRL, 0xf, 0xf, true));
}
DI float row_sum16(float x) {
  x += dpp<0x128>(x); x += dpp<0x124>(x); x += dpp<0x122>(x); x += dpp<0x121>(x); return x;
}
DI float xrow_sum(float x) {
  auto a = __builtin_amdgcn_permlane16_swap(asu(x), asu(x), false, false);
  x = asf(a[0]) + asf(a[1]);
  auto b = __builtin_amdgcn_permlane32_swap(asu(x), asu(x), false, false);
  return asf(b[0]) + asf(b[1]);
}
DI float xrow_max(float x) {
  auto a = __builtin_amdgcn_permlane16_swap(asu(x), asu(x), false, false);
  x = fmaxf(asf(a[0]), asf(a[1]));
  auto b = __builtin_amdgcn_permlane32_swap(asu(x), asu(x), false, false);
  return fmaxf(asf(b[0]), asf(b[1]));
}
DI float ex2(float x) { return __builtin_amdgcn_exp2f(x); }
template <typename T> DI T ntload(const void* p) { return __builtin_nontemporal_load(reinterpret_cast<const T*>(p)); }
#define MFMA32(a, b, c) __builtin_amdgcn_mfma_f32_32x32x16_bf16(__builtin_bit_cast(bf16x8, a), __builtin_bit_cast(bf16x8, b), (c), 0, 0, 0)
DI u32x4 pack8(const f32x16& x, int s) {
  u32x4 p;
#pragma unroll
  for (int j = 0; j < 4; ++j) p[j] = pack2(x[8 * s + 2 * j], x[8 * s + 2 * j + 1]);
  return p;
}
DI int opaque(int x) { asm volatile("" : "+v"(x)); return x; }
#define VM0() asm volatile("s_waitcnt vmcnt(0)" ::: "memory")
#define CBAR() asm volatile("" ::: "memory")
#define WGBAR() do { asm volatile("s_waitcnt lgkmcnt(0)" ::: "memory"); __builtin_amdgcn_s_barrier(); asm volatile("" ::: "memory"); } while (0)

#define STAMP(k) do { } while (0)

typedef __amdgpu_buffer_rsrc_t rsrc_t;
struct Tag { unsigned lo, hi; };
DI Tag load_tag(unsigned char* ws, size_t off) {
  const u64 g = __hip_atomic_load((GAS u64*)(ws + off), RLX_AGENT) + 1ull;
  Tag t;
  t.lo = __builtin_amdgcn_readfirstlane((unsigned)g);
  t.hi = __builtin_amdgcn_readfirstlane((unsigned)(g >> 32));
  return t;
}
DI void st_g(rsrc_t rs, int idx, float v0, float v1, Tag t) {
  const u32x4 x = {asu(v0), asu(v1), t.lo, t.hi};
  __builtin_amdgcn_raw_buffer_store_b128(x, rs, (int)Z_GRAN + idx * 16, 0, 16);
}
DI u32x4 ld_g(rsrc_t rs, int idx) { return __builtin_amdgcn_raw_buffer_load_b128(rs, (int)Z_GRAN + idx * 16, 0, 16); }
DI bool tag_ok(const u32x4& x, Tag t) { return (x[2] == t.lo) & (x[3] == t.hi); }
DI void st_flag(rsrc_t rs, int off, Tag t) {
  const u32x2 x = {t.lo, t.hi};
  __builtin_amdgcn_raw_buffer_store_b64(x, rs, off, 0, 16);
}
DI bool spin_fail(unsigned& spins) {
  __builtin_amdgcn_s_sleep(1);
  return ++spins > kSpinCap;
}
DI void prepoll(rsrc_t rs, int idx, int stride, int n, Tag t, int lane) {
  for (unsigned spins = 0;;) {
    bool ok = true;
    if (lane < n) ok = tag_ok(ld_g(rs, idx + lane * stride), t);
    if (__all(ok)) break;
    __builtin_amdgcn_s_sleep(POLL_SLEEP);
    if (++spins > kSpinCap) break;
  }
  CBAR();
}
template <int N> DI void gather(rsrc_t rs, int idx, int stride, Tag t, float (&v)[N]) {
  for (unsigned spins = 0;;) {
    bool ok = true;
#pragma unroll
    for (int k = 0; k < N; ++k) {
      const u32x4 x = ld_g(rs, idx + k * stride);
      v[k] = asf(x[0]);
      ok &= tag_ok(x, t);
    }
    if (__all(ok)) break;
    if (spin_fail(spins)) break;
  }
  CBAR();
}

DI void fma_row(float (&acc)[12], float p, const u32x4& a, const u32x2& b) {
#pragma unroll
  for (int k = 0; k < 4; ++k) { acc[2 * k] = fmaf(p, bflo(a[k]), acc[2 * k]); acc[2 * k + 1] = fmaf(p, bfhi(a[k]), acc[2 * k + 1]); }
#pragma unroll
  for (int k = 0; k < 2; ++k) { acc[8 + 2 * k] = fmaf(p, bflo(b[k]), acc[8 + 2 * k]); acc[8 + 2 * k + 1] = fmaf(p, bfhi(b[k]), acc[8 + 2 * k + 1]); }
}
DI void rows_to_lds(float (&acc)[12], LAS float* dst, int lane) {
  const int r = lane >> 4, c = lane & 15;
#pragma unroll
  for (int k = 0; k < 12; ++k) acc[k] = xrow_sum(acc[k]);
  if (r == 0) {
#pragma unroll
    for (int k = 0; k < 8; ++k) dst[8 * c + k] = acc[k];
#pragma unroll
    for (int k = 0; k < 4; ++k) dst[128 + 4 * c + k] = acc[8 + k];
  }
}

DI void out_reduce(rsrc_t rs, unsigned char* ws, u16* __restrict__ out, LAS unsigned char* L, int mat, int col0, int tid, Tag t) {
  const int col = tid & 127, qd = tid >> 7;
  float v[8];
  gather<8>(rs, I_OUT + (mat * NH + qd * 8) * DM + col0 + col, DM, t, v);
  LAS float* red = (LAS float*)(L + L_RED);
  red[tid] = ((v[0] + v[1]) + (v[2] + v[3])) + ((v[4] + v[5]) + (v[6] + v[7]));
  WGBAR();
  if (tid < 128) out[mat * DM + col0 + tid] = f2bf((red[tid] + red[128 + tid]) + (red[256 + tid] + red[384 + tid]));
  if (col0 == 0 && tid == 0) st_flag(rs, (int)(mat ? Z_GENB : Z_GENA), t);
}

constexpr int NQ = 5;
struct QLoads { u32x4 a[8]; u32x2 b[8]; float x[8]; };
DI void q_issue(QLoads& Q, const u16* __restrict__ xA, const u16* __restrict__ wqA, const u16* __restrict__ xB,
                const u16* __restrict__ wqB, int h, int jj, int wave, int lane) {
  const int r = lane >> 4, c = lane & 15;
  const u16* x = wave < 4 ? xA : xB;
  const u16* w = wave < 4 ? wqA : wqB;
#pragma unroll
  for (int i = 0; i < 8; ++i) {
    const int row = 128 * jj + 32 * (wave & 3) + 4 * i + r;
    const u16* p = w + (size_t)row * HD + h * DH;
    Q.a[i] = ntload<u32x4>(p + 8 * c); Q.b[i] = ntload<u32x2>(p + 128 + 4 * c);
    Q.x[i] = bflo((unsigned)x[row]);
  }
}
DI void q_partial(QLoads& Q, LAS unsigned char* L, int wave, int lane) {
  float acc[12];
#pragma unroll
  for (int k = 0; k < 12; ++k) acc[k] = 0.f;
#pragma unroll
  for (int i = 0; i < 8; ++i) fma_row(acc, Q.x[i], Q.a[i], Q.b[i]);
  rows_to_lds(acc, (LAS float*)(L + L_RED) + wave * DH, lane);
}
DI void q_publish(rsrc_t rs, LAS unsigned char* L, int h, int jj, int wave, int lane, Tag ta, Tag tb) {
  const int tid = wave * 64 + lane;
  const LAS float* red = (const LAS float*)(L + L_RED);
  if (wave < 6) {
    const int mt = wave >= 3 ? 1 : 0, d = tid - mt * DH;
    const LAS float* rp = red + mt * 4 * DH + d;
    st_g(rs, I_QP + (h * NQ + jj) * 384 + tid, (rp[0] + rp[DH]) + (rp[2 * DH] + rp[3 * DH]), 0.f, mt ? tb : ta);
  }
}
DI void q_gather(rsrc_t rs, LAS unsigned char* L, int h, int part, int wave, int lane, Tag t) {
  const int tid = wave * 64 + lane;
  if (wave < 3) {
    float v[NQ];
    prepoll(rs, I_QP + h * NQ * 384 + part * DH + wave * 64 + 63, 384, NQ, t, lane);
    gather<NQ>(rs, I_QP + h * NQ * 384 + part * DH + tid, 384, t, v);
    ((LAS u16*)(L + L_QS))[tid] = f2bf(((v[0] + v[1]) + (v[2] + v[3])) + v[4]);
  }
}

DI void a_role(const u16* __restrict__ xA, const u16* __restrict__ wqA, const u16* __restrict__ xB, const u16* __restrict__ wqB,
               const u16* __restrict__ K, const u16* __restrict__ V, const u16* __restrict__ woA,
               unsigned char* ws, u16* __restrict__ out, LAS unsigned char* L, int h, int j, int wave, int lane) {
  const int r = lane >> 4, c = lane & 15, tid = wave * 64 + lane;
  STAMP(0);
  const rsrc_t rs = __builtin_amdgcn_make_buffer_rsrc((void*)ws, 0, (int)W_END, 0x00020000);
  const Tag ta = load_tag(ws, Z_GENA), tb = load_tag(ws, Z_GENB);
  QLoads Q;
  if (j < NQ) q_issue(Q, xA, wqA, xB, wqB, h, j, wave, lane);
  constexpr int KR = 2, TK = 4 * KR, NTH = SK / TK, NR = NTH / (8 * NA);
  const int T0 = (NTH * j) / NA, T1 = (NTH * (j + 1)) / NA;
  const int t_beg = T0 + wave, t_x = T0 + 8 * NR + wave;
  const bool extra = t_x < T1;
  const size_t base = ((size_t)h * SK + r) * DH;
  const u16* kp = K + base;
  const u16* vp = V + base;
  u32x4 ka[2][KR], va[2][KR]; u32x2 kb[2][KR], vb[2][KR];
#define LOADT(b, t) do { _Pragma("unroll") for (int i = 0; i < KR; ++i) { \
    const u16* kr = kp + (size_t)((t) * TK + i * 4) * DH; const u16* vr = vp + (size_t)((t) * TK + i * 4) * DH; \
    ka[b][i] = ntload<u32x4>(kr + 8 * c); kb[b][i] = ntload<u32x2>(kr + 128 + 4 * c); \
    va[b][i] = ntload<u32x4>(vr + 8 * c); vb[b][i] = ntload<u32x2>(vr + 128 + 4 * c); } } while (0)
#define PROC(b) do { \
      float s_[KR]; float mn_ = m; \
      _Pragma("unroll") for (int q = 0; q < KR; ++q) { \
        float d = 0.f; \
        _Pragma("unroll") for (int k = 0; k < 4; ++k) d = dot2(ka[b][q][k], qa[k], d); \
        d = dot2(kb[b][q][0], qb[0], d); d = dot2(kb[b][q][1], qb[1], d); \
        d = row_sum16(d); \
        s_[q] = rbf(rbf(d) * SCALE); \
        mn_ = fmaxf(mn_, s_[q]); \
      } \
      const float alpha_ = ex2((m - mn_) * LOG2E); \
      m = mn_; \
      float ps_ = 0.f; \
      _Pragma("unroll") for (int q = 0; q < KR; ++q) { s_[q] = ex2((s_[q] - mn_) * LOG2E); ps_ += s_[q]; } \
      l = l * alpha_ + ps_; \
      _Pragma("unroll") for (int k = 0; k < 12; ++k) acc[k] *= alpha_; \
      _Pragma("unroll") for (int q = 0; q < KR; ++q) fma_row(acc, s_[q], va[b][q], vb[b][q]); \
    } while (0)
  if (j < NQ) q_partial(Q, L, wave, lane);
  STAMP(10);
  WGBAR();
  STAMP(11);
  __builtin_amdgcn_sched_barrier(0);
  if (wave >= EARLY_W) LOADT(0, t_beg);
  __builtin_amdgcn_sched_barrier(0);
  if (j < NQ) q_publish(rs, L, h, j, wave, lane, ta, tb);
  q_gather(rs, L, h, 0, wave, lane, ta);
  STAMP(12);
  __builtin_amdgcn_sched_barrier(0);
  if (wave < EARLY_W) LOADT(0, t_beg);
  __builtin_amdgcn_sched_barrier(0);
  WGBAR();
  STAMP(1);
  const u32x4 qa = *(const LAS u32x4*)(L + L_QS + 16 * c);
  const u32x2 qb = *(const LAS u32x2*)(L + L_QS + 256 + 8 * c);
  float m = -INFINITY, l = 0.f, acc[12];
#pragma unroll
  for (int k = 0; k < 12; ++k) acc[k] = 0.f;
  for (int rd = 1; rd < NR; ++rd) {
    PROC(0);
    LOADT(0, t_beg + 8 * rd);
  }
  if (extra) LOADT(1, t_x);
  PROC(0);
  if (extra) PROC(1);
#undef PROC
#undef LOADT
  STAMP(2);
  u32x4 wv[6];
  if (j < 5) {
#pragma unroll
    for (int i = 0; i < 6; ++i) wv[i] = ntload<u32x4>(woA + (size_t)(h * DH + 24 * wave + 4 * i + r) * DM + 128 * j + 8 * c);
  }
  const float M = xrow_max(m);
  const float f = ex2((m - M) * LOG2E);
  l = xrow_sum(l * f);
#pragma unroll
  for (int k = 0; k < 12; ++k) acc[k] *= f;
  LAS float* red = (LAS float*)(L + L_RED);
  rows_to_lds(acc, red + wave * 200 + 8, lane);
  if (lane == 0) { red[wave * 200] = M; red[wave * 200 + 1] = l; }
  STAMP(6);
  WGBAR();
  STAMP(7);
  {
    float mw[8], MM = -INFINITY;
#pragma unroll
    for (int w = 0; w < 8; ++w) { mw[w] = red[w * 200]; MM = fmaxf(MM, mw[w]); }
    const int idx = tid < DH ? 8 + tid : 1;
    float s = 0.f;
#pragma unroll
    for (int w = 0; w < 8; ++w) s = fmaf(ex2((mw[w] - MM) * LOG2E), red[w * 200 + idx], s);
    const int ga = I_AP + (h * NA + j) * 200;
    if (tid < DH) st_g(rs, ga + 8 + tid, s, 0.f, ta);
    if (tid == DH) st_g(rs, ga, MM, s, ta);
  }
  STAMP(8);
  if (j < 5) {
    if (wave < 3) {
      float v[NA], mm[NA], ll[NA];
      const int gh = I_AP + h * NA * 200;
      for (unsigned spins = 0;;) {
        bool ok = true;
#pragma unroll
        for (int k = 0; k < NA; ++k) {
          const u32x4 x = ld_g(rs, gh + k * 200 + 8 + tid), y = ld_g(rs, gh + k * 200);
          v[k] = asf(x[0]); mm[k] = asf(y[0]); ll[k] = asf(y[1]);
          ok &= tag_ok(x, ta) & tag_ok(y, ta);
        }
        if (__all(ok)) break;
        if (spin_fail(spins)) break;
      }
      CBAR();
      float Mx = mm[0];
#pragma unroll
      for (int k = 1; k < NA; ++k) Mx = fmaxf(Mx, mm[k]);
      float Lsum = 0.f, o = 0.f;
#pragma unroll
      for (int k = 0; k < NA; ++k) { const float g = ex2((mm[k] - Mx) * LOG2E); Lsum = fmaf(g, ll[k], Lsum); o = fmaf(g, v[k], o); }
      ((LAS float*)(L + L_VEC))[tid] = rbf(o / Lsum);
    }
    WGBAR();
    STAMP(3);
    const LAS float* vec = (const LAS float*)(L + L_VEC);
    float a8[8];
#pragma unroll
    for (int k = 0; k < 8; ++k) a8[k] = 0.f;
#pragma unroll
    for (int i = 0; i < 6; ++i) {
      const float x = vec[24 * wave + 4 * i + r];
#pragma unroll
      for (int k = 0; k < 4; ++k) { a8[2 * k] = fmaf(x, bflo(wv[i][k]), a8[2 * k]); a8[2 * k + 1] = fmaf(x, bfhi(wv[i][k]), a8[2 * k + 1]); }
    }
#pragma unroll
    for (int k = 0; k < 8; ++k) a8[k] = xrow_sum(a8[k]);
    LAS float* r2 = (LAS float*)(L + L_BIG);
    if (r == 0) {
#pragma unroll
      for (int k = 0; k < 8; ++k) r2[wave * 128 + 8 * c + k] = a8[k];
    }
    WGBAR();
    if (tid < 128) {
      float s = 0.f;
#pragma unroll
      for (int w = 0; w < 8; ++w) s += r2[w * 128 + tid];
      st_g(rs, I_OUT + h * DM + 128 * j + tid, s, 0.f, ta);
    }
    STAMP(4);
    if (h == 4 * j + 1) out_reduce(rs, ws, out, L, 0, 128 * j, tid, ta);
    STAMP(5);
  }
}

DI void b_role(const u16* __restrict__ xA, const u16* __restrict__ wqA, const u16* __restrict__ xB, const u16* __restrict__ wqB,
               const u16* __restrict__ ckv, const u16* __restrict__ wuk, const u16* __restrict__ wuv, const u16* __restrict__ woB,
               unsigned char* ws, u16* __restrict__ out, LAS unsigned char* L, int h, int half, int wave, int lane) {
  const int r = lane >> 4, c = lane & 15, tid = wave * 64 + lane;
  const int cl = lane & 31, hh = lane >> 5;
  const int kb_ = 2 * h + half;
  const rsrc_t rs = __builtin_amdgcn_make_buffer_rsrc((void*)ws, 0, (int)W_END, 0x00020000);
  STAMP(0);
  const Tag tb = load_tag(ws, Z_GENB);
  u32x4 cf[4][6];
  {
    u32x4 ua[12]; u32x2 ub[12];
#define LOADU() do { _Pragma("unroll") for (int i = 0; i < 12; ++i) { \
      const u16* row = wuk + ((size_t)(384 * half + 48 * wave + 4 * i + r) * NH + h) * DH; \
      ua[i] = ntload<u32x4>(row + 8 * c); ub[i] = ntload<u32x2>(row + 128 + 4 * c); } } while (0)
#define LOADC() do { _Pragma("unroll") for (int t = 0; t < 4; ++t) _Pragma("unroll") for (int i = 0; i < 6; ++i) \
        cf[t][i] = ntload<u32x4>(ckv + (size_t)(128 * kb_ + 32 * t) * DC + (size_t)(384 * wave + 64 * i + lane) * 8); } while (0)
    LOADU();
    __builtin_amdgcn_sched_barrier(0);
    if (wave >= 3) LOADC();
    __builtin_amdgcn_sched_barrier(0);
    q_gather(rs, L, h, 1, wave, lane, tb);
    STAMP(15);
    __builtin_amdgcn_sched_barrier(0);
    if (wave == 2) LOADC();
    __builtin_amdgcn_sched_barrier(0);
    WGBAR();
    STAMP(1);
    const u32x4 qa = *(const LAS u32x4*)(L + L_QS + 16 * c);
    const u32x2 qb = *(const LAS u32x2*)(L + L_QS + 256 + 8 * c);
    LAS float* qv = (LAS float*)(L + L_VEC);
#pragma unroll
    for (int i = 0; i < 12; ++i) {
      float s = 0.f;
#pragma unroll
      for (int k = 0; k < 4; ++k) s = dot2(ua[i][k], qa[k], s);
      s = dot2(ub[i][0], qb[0], s); s = dot2(ub[i][1], qb[1], s);
      s = row_sum16(s);
      if (c == 0) qv[48 * wave + 4 * i + r] = s;
    }
    WGBAR();
    if (wave < 2) {
      if (lane < 48) {
        u32x4 o;
#pragma unroll
        for (int k = 0; k < 4; ++k) {
          const float a = qv[8 * lane + 2 * k], b = qv[8 * lane + 2 * k + 1];
          const float ah = rbf(a), bh = rbf(b);
          o[k] = wave ? pack2(a - ah, b - bh) : pack2(ah, bh);
        }
        const int c8 = 48 * half + lane;
        __builtin_amdgcn_raw_buffer_store_b128(o, rs, (int)(wave ? W_QL : W_QH) + ((c8 >> 1) * 64 + h + 32 * (c8 & 1)) * 16, 0, 16);
      }
      VM0();
      if (lane == 0) st_flag(rs, (int)Z_FQABS + (2 * kb_ + wave) * 8, tb);
      __builtin_amdgcn_sched_barrier(0);
      LOADC();
    }
#undef LOADC
#undef LOADU
  }
  STAMP(2);
#pragma unroll
  for (int rd = 0; rd < 2; ++rd) {
#pragma unroll
    for (int tt = 0; tt < 2; ++tt)
#pragma unroll
      for (int i = 0; i < 6; ++i) {
        const int e = 384 * wave + 64 * i + lane;
        *(LAS u32x4*)(L + L_BIG + tt * 49664 + (e / 96) * 1552 + (e % 96) * 16) = cf[2 * rd + tt][i];
      }
    WGBAR();
#pragma unroll
    for (int tt = 0; tt < 2; ++tt)
#pragma unroll
      for (int s = 0; s < 6; ++s)
        cf[2 * rd + tt][s] = *(const LAS u32x4*)(L + L_BIG + tt * 49664 + cl * 1552 + (96 * wave + 16 * s + 8 * hh) * 2);
    WGBAR();
  }
  {
    if (wave == 0) {
      for (unsigned spins = 0;;) {
        const u32x4 f = __builtin_amdgcn_raw_buffer_load_b128(rs, (int)Z_FQABS + lane * 16, 0, 16);
        if (__all((f[0] == tb.lo) & (f[1] == tb.hi) & (f[2] == tb.lo) & (f[3] == tb.hi))) break;
        __builtin_amdgcn_s_sleep(POLL_SLEEP);
        if (++spins > kSpinCap) break;
      }
    }
    WGBAR();
    STAMP(3);
    u32x4 qh[6], ql[6];
#pragma unroll
    for (int s = 0; s < 6; ++s) {
      qh[s] = __builtin_amdgcn_raw_buffer_load_b128(rs, (int)W_QH + ((wave * 6 + s) * 64 + lane) * 16, 0, 16);
      ql[s] = __builtin_amdgcn_raw_buffer_load_b128(rs, (int)W_QL + ((wave * 6 + s) * 64 + lane) * 16, 0, 16);
    }
    __builtin_amdgcn_sched_barrier(0);
    f32x16 zero;
#pragma unroll
    for (int i = 0; i < 16; ++i) zero[i] = 0.f;
    LAS f32x4* sp = (LAS f32x4*)(L + L_BIG);
#pragma unroll
    for (int t = 0; t < 4; ++t) {
      f32x16 S = zero;
#pragma unroll
      for (int s = 0; s < 6; ++s) S = MFMA32(cf[t][s], ql[s], S);
#pragma unroll
      for (int s = 0; s < 6; ++s) S = MFMA32(cf[t][s], qh[s], S);
#pragma unroll
      for (int g = 0; g < 4; ++g) sp[((wave * 4 + t) * 4 + g) * 64 + lane] = f32x4{S[4 * g], S[4 * g + 1], S[4 * g + 2], S[4 * g + 3]};
    }
    STAMP(4);
    WGBAR();
    float sv[8];
    {
      const int t = wave >> 1;
#pragma unroll
      for (int gg = 0; gg < 2; ++gg) {
        const int g = 2 * (wave & 1) + gg;
        f32x4 a = sp[((0 * 4 + t) * 4 + g) * 64 + lane];
#pragma unroll
        for (int w = 1; w < 8; ++w) a += sp[((w * 4 + t) * 4 + g) * 64 + lane];
#pragma unroll
        for (int k = 0; k < 4; ++k) sv[4 * gg + k] = rbf(rbf(a[k]) * SCALE);
      }
    }
    float m = fmaxf(fmaxf(fmaxf(sv[0], sv[1]), fmaxf(sv[2], sv[3])), fmaxf(fmaxf(sv[4], sv[5]), fmaxf(sv[6], sv[7])));
    m = fmaxf(m, __shfl_xor(m, 32));
    LAS float* mx = (LAS float*)(L + L_RED);
    if (lane < 32) mx[wave * 32 + lane] = m;
    WGBAR();
#pragma unroll
    for (int w = 0; w < 8; ++w) m = fmaxf(m, mx[w * 32 + cl]);
    float l = 0.f;
#pragma unroll
    for (int k = 0; k < 8; ++k) { sv[k] = ex2((sv[k] - m) * LOG2E); l += sv[k]; }
    l += __shfl_xor(l, 32);
    if (lane < 32) mx[256 + wave * 32 + lane] = l;
    LAS u32x4* pf = (LAS u32x4*)(L + L_SF);
    pf[wave * 64 + lane] = u32x4{pack2(sv[0], sv[1]), pack2(sv[2], sv[3]), pack2(sv[4], sv[5]), pack2(sv[6], sv[7])};
    WGBAR();
    u32x4 pb[4][2];
#pragma unroll
    for (int t = 0; t < 4; ++t) { pb[t][0] = pf[(2 * t) * 64 + lane]; pb[t][1] = pf[(2 * t + 1) * 64 + lane]; }
    if (wave == 0 && lane < 32) {
      float ls = 0.f;
#pragma unroll
      for (int w = 0; w < 8; ++w) ls += mx[256 + w * 32 + lane];
      const u32x2 ml = {asu(m), asu(ls)};
      __builtin_amdgcn_raw_buffer_store_b64(ml, rs, (int)W_BML + ((cl * NSB + kb_) * 2) * 4, 0, 16);
    }
    u32x4 idlo, idhi;
#pragma unroll
    for (int k = 0; k < 4; ++k) {
      const int k0 = 8 * hh + 2 * k, k1 = k0 + 1;
      idlo[k] = (cl == k0 ? 0x3f80u : 0u) | (cl == k1 ? 0x3f800000u : 0u);
      idhi[k] = (cl == k0 + 16 ? 0x3f80u : 0u) | (cl == k1 + 16 ? 0x3f800000u : 0u);
    }
    STAMP(5);
    LAS float* ost = (LAS float*)(L + L_BIG) + wave * 3200;
#pragma unroll
    for (int ct = 0; ct < 3; ++ct) {
      f32x16 O = zero;
#pragma unroll
      for (int t = 0; t < 4; ++t) {
        f32x16 T = MFMA32(cf[t][2 * ct], idlo, zero);
        T = MFMA32(cf[t][2 * ct + 1], idhi, T);
        O = MFMA32(pack8(T, 0), pb[t][0], O);
        O = MFMA32(pack8(T, 1), pb[t][1], O);
      }
#pragma unroll
      for (int g = 0; g < 4; ++g)
        *(LAS f32x4*)(ost + cl * 100 + 32 * ct + 8 * g + 4 * hh) = f32x4{O[4 * g], O[4 * g + 1], O[4 * g + 2], O[4 * g + 3]};
    }
    asm volatile("s_waitcnt lgkmcnt(0)" ::: "memory");
#pragma unroll
    for (int i = 0; i < 6; ++i) {
      const int e = i * 64 + lane, hd = e / 12, q8 = e % 12;
      const f32x4 v0 = *(const LAS f32x4*)(ost + hd * 100 + 8 * q8), v1 = *(const LAS f32x4*)(ost + hd * 100 + 8 * q8 + 4);
      const u32x4 o = {pack2(v0[0], v0[1]), pack2(v0[2], v0[3]), pack2(v1[0], v1[1]), pack2(v1[2], v1[3])};
      __builtin_amdgcn_raw_buffer_store_b128(o, rs, (int)W_BO + ((kb_ * NH + hd) * DC + 96 * wave + 8 * q8) * 2, 0, 16);
    }
    STAMP(6);
    VM0();
    WGBAR();
    if (tid == 0) st_flag(rs, (int)Z_FBATT + kb_ * 8, tb);
    STAMP(7);
  }
  {
    const int lane2 = opaque(lane);
    const int r = lane2 >> 4, c = lane2 & 15, tid = wave * 64 + lane2;
    u32x4 va[12]; u32x2 vb[12];
#pragma unroll
    for (int i = 0; i < 12; ++i) {
      const u16* row = wuv + ((size_t)(384 * half + 48 * wave + 4 * i + r) * NH + h) * DH;
      va[i] = ntload<u32x4>(row + 8 * c); vb[i] = ntload<u32x2>(row + 128 + 4 * c);
    }
    __builtin_amdgcn_sched_barrier(0);
    if (wave == 0) {
      for (unsigned spins = 0;;) {
        const u32x2 f = __builtin_amdgcn_raw_buffer_load_b64(rs, (int)Z_FBATT + lane2 * 8, 0, 16);
        if (__all((f[0] == tb.lo) & (f[1] == tb.hi))) break;
        __builtin_amdgcn_s_sleep(POLL_SLEEP);
        if (++spins > kSpinCap) break;
      }
    }
    WGBAR();
    STAMP(8);
    const u32x2 st = __builtin_amdgcn_raw_buffer_load_b64(rs, (int)W_BML + (h * NSB + lane2) * 8, 0, 16);
    u32x4 pa[8];
    const int l48 = lane2 < 48 ? lane2 : 0;
#pragma unroll
    for (int i = 0; i < 8; ++i)
      pa[i] = __builtin_amdgcn_raw_buffer_load_b128(rs, (int)W_BO + (((8 * wave + i) * NH + h) * DC + 384 * half + 8 * l48) * 2, 0, 16);
    const float m0 = asf(st[0]), l0 = asf(st[1]);
    float M = m0;
#pragma unroll
    for (int o = 1; o < 64; o <<= 1) M = fmaxf(M, __shfl_xor(M, o));
    const float f0 = ex2((m0 - M) * LOG2E);
    float Ls = f0 * l0;
#pragma unroll
    for (int o = 1; o < 64; o <<= 1) Ls += __shfl_xor(Ls, o);
    const float w0 = f0 / Ls;
    float sa[8];
#pragma unroll
    for (int k = 0; k < 8; ++k) sa[k] = 0.f;
#pragma unroll
    for (int i = 0; i < 8; ++i) {
      const float w = __shfl(w0, 8 * wave + i);
#pragma unroll
      for (int k = 0; k < 4; ++k) { sa[2 * k] = fmaf(w, bflo(pa[i][k]), sa[2 * k]); sa[2 * k + 1] = fmaf(w, bfhi(pa[i][k]), sa[2 * k + 1]); }
    }
    LAS f32x4* acc4 = (LAS f32x4*)(L + L_RED);
    if (lane2 < 48) {
      acc4[wave * 96 + 2 * lane2] = f32x4{sa[0], sa[1], sa[2], sa[3]};
      acc4[wave * 96 + 2 * lane2 + 1] = f32x4{sa[4], sa[5], sa[6], sa[7]};
    }
    __builtin_amdgcn_sched_barrier(0);
    u32x4 wo[24];
    const int lc = lane2 < 40 ? lane2 : 0;
#pragma unroll
    for (int i = 0; i < 24; ++i) wo[i] = ntload<u32x4>(woB + (size_t)(h * DH + 24 * wave + i) * DM + 320 * half + 8 * lc);
    __builtin_amdgcn_sched_barrier(0);
    WGBAR();
    if (tid < 384) {
      const LAS float* af = (const LAS float*)(L + L_RED) + tid;
      float s = 0.f;
#pragma unroll
      for (int w = 0; w < 8; ++w) s += af[w * 384];
      ((LAS float*)(L + L_VEC))[tid] = s;
    }
    WGBAR();
    STAMP(9);
    float acc[12];
#pragma unroll
    for (int k = 0; k < 12; ++k) acc[k] = 0.f;
    const LAS float* cv = (const LAS float*)(L + L_VEC);
#pragma unroll
    for (int i = 0; i < 12; ++i) fma_row(acc, cv[48 * wave + 4 * i + r], va[i], vb[i]);
    LAS float* red = (LAS float*)(L + L_RED);
    rows_to_lds(acc, red + wave * DH, lane2);
    WGBAR();
    if (tid < DH) {
      float s = 0.f;
#pragma unroll
      for (int w = 0; w < 8; ++w) s += red[w * DH + tid];
      st_g(rs, I_CTX + (h * 2 + half) * DH + tid, s, 0.f, tb);
      float v[1];
      gather<1>(rs, I_CTX + (h * 2 + (half ^ 1)) * DH + tid, 0, tb, v);
      ((LAS float*)(L + L_VEC))[tid] = rbf(half ? v[0] + s : s + v[0]);
    }
    WGBAR();
    STAMP(10);
    float a8[8];
#pragma unroll
    for (int k = 0; k < 8; ++k) a8[k] = 0.f;
#pragma unroll
    for (int i = 0; i < 24; ++i) {
      const float x = cv[24 * wave + i];
#pragma unroll
      for (int k = 0; k < 4; ++k) { a8[2 * k] = fmaf(x, bflo(wo[i][k]), a8[2 * k]); a8[2 * k + 1] = fmaf(x, bfhi(wo[i][k]), a8[2 * k + 1]); }
    }
    LAS float* r2 = (LAS float*)(L + L_BIG);
    if (lane2 < 40) {
#pragma unroll
      for (int k = 0; k < 8; ++k) r2[wave * 320 + 8 * lane2 + k] = a8[k];
    }
    WGBAR();
    if (tid < 320) {
      float s = 0.f;
#pragma unroll
      for (int w = 0; w < 8; ++w) s += r2[w * 320 + tid];
      st_g(rs, I_OUT + (NH + h) * DM + 320 * half + tid, s, 0.f, tb);
    }
    STAMP(11);
    if ((kb_ % 12) == 3 && kb_ < 60) out_reduce(rs, ws, out, L, 1, 128 * (kb_ / 12), tid, tb);
    STAMP(12);
  }
}

__global__ void __launch_bounds__(512, 2) mega(const u16* __restrict__ xA, const u16* __restrict__ K, const u16* __restrict__ V,
                                               const u16* __restrict__ wqA, const u16* __restrict__ woA,
                                               const u16* __restrict__ xB, const u16* __restrict__ ckv, const u16* __restrict__ wqB,
                                               const u16* __restrict__ wuk, const u16* __restrict__ wuv, const u16* __restrict__ woB,
                                               unsigned char* ws, u16* __restrict__ out) {
  extern __shared__ __attribute__((aligned(16))) unsigned char smem[];
  LAS unsigned char* L = (LAS unsigned char*)smem;
  const int tid = threadIdx.x, lane = tid & 63;
  const int wave = __builtin_amdgcn_readfirstlane(tid >> 6);
  const int b = blockIdx.x;
  const int h = (b & 7) * 4 + (b >> 6), jj = (b >> 3) & 7;
  if (jj < NA) a_role(xA, wqA, xB, wqB, K, V, woA, ws, out, L, h, jj, wave, lane);
  else b_role(xA, wqA, xB, wqB, ckv, wuk, wuv, woB, ws, out, L, h, jj - NA, wave, lane);
}

extern "C" void kernel_launch(void* const* d_in, const int* in_sizes, int n_in,
                              void* d_out, int out_size, void* d_ws, size_t ws_size,
                              hipStream_t stream) {
  (void)hipFuncSetAttribute((const void*)mega, hipFuncAttributeMaxDynamicSharedMemorySize, L_BYTES);
  mega<<<256, 512, L_BYTES, stream>>>((const u16*)d_in[0], (const u16*)d_in[1], (const u16*)d_in[2], (const u16*)d_in[3], (const u16*)d_in[4],
                                      (const u16*)d_in[5], (const u16*)d_in[6], (const u16*)d_in[7], (const u16*)d_in[8], (const u16*)d_in[9],
                                      (const u16*)d_in[10], (unsigned char*)d_ws, (u16*)d_out);
}
```
